```python
import torch

def _softmax(x, axis=-1):
    return torch.softmax(x, dim=axis)
def _gelu(x):
    return torch.nn.functional.gelu(x, approximate="tanh")
def _silu(x):
    return torch.nn.functional.silu(x)
def _sigmoid(x):
    return torch.sigmoid(x)
def _layernorm(x, axis=-1, eps=1e-5):
    m = x.mean(dim=axis, keepdim=True)
    v = x.var(dim=axis, keepdim=True, unbiased=False)
    return (x - m) / torch.sqrt(v + eps)
def _scatter_add(tgt, idx, src, axis=0):
    return tgt.clone().index_add_(axis, idx, src)
def _segment_sum(src, seg, n):
    out = torch.zeros((n,) + src.shape[1:], dtype=src.dtype)
    return out.index_add_(0, seg, src)
def _pad(x, pw):
    flat = tuple(v for pair in reversed(pw) for v in pair)
    return torch.nn.functional.pad(x, flat)

kernel_name = 'parallel_sum__ssm_conv_h3__moe_arch_mod__prompt'

def setup_inputs():

    _w = (lambda a: (lambda t: t.to(torch.bfloat16) if t.is_floating_point() else t)(torch.as_tensor(a)))
    def _A_setup():
        B, S, D = 1, 2048, 640
        ks = 4
        return {
            "q": _w((lambda *s: torch.randn(*s, dtype=torch.bfloat16) * 0.05)(B, S, D)),
            "k": _w((lambda *s: torch.randn(*s, dtype=torch.bfloat16) * 0.4)(B, S, D)),
            "v": _w((lambda *s: torch.randn(*s, dtype=torch.bfloat16) * 0.4)(B, S, D)),
            "shift_k": _w((lambda *s: torch.randn(*s, dtype=torch.bfloat16) * 0.4)(D, ks) * 0.5),
            "ssm_k": _w((lambda *s: torch.randn(*s, dtype=torch.bfloat16) * 1.6)(D, S) * (1.0 / S ** 0.5)),
            "ssm_D": _w((lambda *s: torch.randn(*s, dtype=torch.bfloat16) * 0.4)(D)),
        }
    _A_in = _A_setup()

    def _B_setup():
        B, S, D, F, C = 1, 2048, 640, 1728, 2
        return {
            "x": _w((lambda *s: torch.randn(*s, dtype=torch.bfloat16) * 0.05)(B, S, D)),
            "w_r": _w((lambda *s: torch.randn(*s, dtype=torch.bfloat16) * 0.05)(D)),
            "w_gate": _w((lambda *s: torch.randn(*s, dtype=torch.bfloat16) * 0.05)(D, F)),
            "w_up": _w((lambda *s: torch.randn(*s, dtype=torch.bfloat16) * 0.05)(D, F)),
            "w_down": _w((lambda *s: torch.randn(*s, dtype=torch.bfloat16) * 0.05)(F, D)),
        }
    _B_in = _B_setup()

    return {
        "A_q": _A_in["q"],
        "A_k": _A_in["k"],
        "A_v": _A_in["v"],
        "A_shift_k": _A_in["shift_k"],
        "A_ssm_k": _A_in["ssm_k"],
        "A_ssm_D": _A_in["ssm_D"],
        "B_x": _B_in["x"],
        "B_w_r": _B_in["w_r"],
        "B_w_gate": _B_in["w_gate"],
        "B_w_up": _B_in["w_up"],
        "B_w_down": _B_in["w_down"],
    }

def reference(A_q, A_k, A_v, A_shift_k, A_ssm_k, A_ssm_D, B_x, B_w_r, B_w_gate, B_w_up, B_w_down):
    def _A_ref(q, k, v, shift_k, ssm_k, ssm_D):
        B, S, D = q.shape
        ks = shift_k.shape[-1]
        k_pad = _pad(k, ((0, 0), (ks - 1, 0), (0, 0)))
        k_shift = torch.zeros_like(k)
        for t in range(ks):
            k_shift = k_shift + k_pad[:, t:t + S, :] * shift_k[:, ks - 1 - t][None, None, :]
        kv = k_shift * v
        L = 2 * S
        kv_pad = _pad(kv, ((0, 0), (0, S), (0, 0)))
        kf_pad = _pad(ssm_k, ((0, 0), (0, S)))
        kv_f = (lambda x, axes=(-2,-1): torch.fft.rfft2(x.float(), dim=axes))(kv_pad[:, None, :, :], axes=(1, 2))
        kf_f = (lambda x, axes=(-2,-1): torch.fft.rfft2(x.float(), dim=axes))(kf_pad[:, None, :], axes=(1, 2))
        kf_f = (lambda x, *d: x.permute(*d))(kf_f, 1, 2, 0)
        y_f = kv_f * kf_f[None, :, :, :]
        y = (lambda x, s, axes=(-2,-1): torch.fft.irfft2(x, s=s, dim=axes).to(torch.bfloat16))(y_f, (1, L), axes=(1, 2))[:, 0, :S, :]
        y = y + kv * ssm_D[None, None, :]
        return q * y

    def _B_ref(x, w_r, w_gate, w_up, w_down):
        B, S, D = x.shape
        C = 2
        r = x @ w_r
        idx = torch.argsort(-r, axis=-1)[:, :C]
        idx_e = (lambda x, n, axis: torch.repeat_interleave(x, n, dim=axis))(idx.reshape(B, C, 1), D, axis=2)
        x_sel = (lambda x, i, axis: torch.take_along_dim(x, i, dim=axis))(x, idx_e, axis=1)
        g = _silu(x_sel @ w_gate)
        u = x_sel @ w_up
        y = (g * u) @ w_down
        r_sel = _sigmoid((lambda x, i, axis: torch.take_along_dim(x, i, dim=axis))(r, idx, axis=1))
        y = y * r_sel.reshape(B, C, 1)
        b_off = (torch.arange(B) * S).reshape(B, 1)
        flat_idx = (idx + b_off).reshape(B * C)
        zero = (x * 0.0).reshape(B * S, D)
        upd = _scatter_add(zero, flat_idx, y.reshape(B * C, D), axis=0)
        return x + upd.reshape(B, S, D)

    x = A_q
    xn = _layernorm(x, axis=-1)
    return x + _A_ref(xn, A_k, A_v, A_shift_k, A_ssm_k, A_ssm_D) + _B_ref(xn, B_w_r, B_w_gate, B_w_up, B_w_down)
```

```cpp
#include <hip/hip_runtime.h>
#include <hip/hip_bf16.h>
#include <stdint.h>

constexpr int S = 2048, D = 640, F = 1728;
typedef unsigned short u16;
__device__ __forceinline__ float bf2f(u16 x) { return __uint_as_float(((unsigned)x) << 16); }
__device__ __forceinline__ u16 f2bf(float f) { __hip_bfloat16 b = __float2bfloat16(f); return *(u16*)&b; }
__device__ __forceinline__ float rbf(float f) { return bf2f(f2bf(f)); }

__global__ void k_stats(const u16* q, const u16* wr, float* mean, float* den, float* r) {
    int row = blockIdx.x * 4 + (threadIdx.x >> 6), lane = threadIdx.x & 63;
    float x[10]; float s = 0;
    for (int i = 0; i < 10; i++) { x[i] = bf2f(q[row * D + lane + 64 * i]); s += x[i]; }
    for (int o = 32; o; o >>= 1) s += __shfl_xor(s, o);
    float m = s / D; float v = 0;
    for (int i = 0; i < 10; i++) { float d = x[i] - m; v += d * d; }
    for (int o = 32; o; o >>= 1) v += __shfl_xor(v, o);
    v = rbf(v / D); float mb = rbf(m);
    float dn = rbf(sqrtf(rbf(v + 1e-5f)));
    float acc = 0;
    for (int i = 0; i < 10; i++) { float xn = rbf(rbf(x[i] - mb) / dn); acc += xn * bf2f(wr[lane + 64 * i]); }
    for (int o = 32; o; o >>= 1) acc += __shfl_xor(acc, o);
    if (lane == 0) { mean[row] = mb; den[row] = dn; r[row] = rbf(acc); }
}
__global__ void k_conv(const u16* q, const u16* k, const u16* v, const u16* sk, const u16* h, const u16* Dp,
                       const float* mean, const float* den, u16* out, float* xa) {
    __shared__ float u[S], hh[S];
    int d = blockIdx.x;
    float w[4]; for (int i = 0; i < 4; i++) w[i] = bf2f(sk[d * 4 + i]);
    for (int t = threadIdx.x; t < S; t += blockDim.x) {
        float ks = 0;
        for (int i = 0; i < 4; i++) { int tt = t - 3 + i; float kk = tt >= 0 ? bf2f(k[tt * D + d]) : 0.f; ks = rbf(ks + rbf(kk * w[3 - i])); }
        u[t] = rbf(ks * bf2f(v[t * D + d]));
        hh[t] = bf2f(h[d * S + t]);
    }
    __syncthreads();
    float Dd = bf2f(Dp[d]);
    for (int t = threadIdx.x; t < S; t += blockDim.x) {
        float a = 0;
        for (int s = 0; s <= t; s++) a += hh[t - s] * u[s];
        float y = rbf(rbf(a) + rbf(u[t] * Dd));
        float x = bf2f(q[t * D + d]);
        float xn = rbf(rbf(x - mean[t]) / den[t]);
        float xA = rbf(x + rbf(xn * y));
        xa[t * D + d] = xA;
        out[t * D + d] = f2bf(xA + xn);
    }
}
__device__ __forceinline__ void top2(const float* r, int& i0o, int& i1o) {
    float b0 = -INFINITY, b1 = -INFINITY; int i0 = 0, i1 = 1;
    for (int t = 0; t < S; t++) { float x = r[t]; bool g0 = x > b0, g1 = x > b1;
        b1 = g0 ? b0 : (g1 ? x : b1); i1 = g0 ? i0 : (g1 ? t : i1); b0 = g0 ? x : b0; i0 = g0 ? t : i0; }
    i0o = i0; i1o = i1;
}
__global__ void k_ffn1(const u16* q, const float* mean, const float* den, const float* r, const u16* wg, const u16* wu, float* hbuf) {
    int i0, i1; top2(r, i0, i1);
    int f = blockIdx.x * blockDim.x + threadIdx.x;
    if (f >= F) return;
    _Pragma("unroll") for (int c = 0; c < 2; c++) {
        int t = c ? i1 : i0; float g = 0, u = 0;
        for (int d = 0; d < D; d++) { float xn = rbf(rbf(bf2f(q[t * D + d]) - mean[t]) / den[t]); g += xn * bf2f(wg[d * F + f]); u += xn * bf2f(wu[d * F + f]); }
        g = rbf(g); u = rbf(u);
        g = rbf(g / (1.f + expf(-g)));
        hbuf[c * F + f] = rbf(g * u);
    }
}
__global__ void k_ffn2(const u16* q, const float* mean, const float* den, const float* r, const u16* wd, const float* hbuf, const float* xa, u16* out) {
    int i0, i1; top2(r, i0, i1);
    int d = blockIdx.x * blockDim.x + threadIdx.x;
    if (d >= D) return;
    _Pragma("unroll") for (int c = 0; c < 2; c++) {
        int t = c ? i1 : i0; float y = 0;
        for (int f = 0; f < F; f++) y += hbuf[c * F + f] * bf2f(wd[f * D + d]);
        y = rbf(y);
        float sg = rbf(1.f / (1.f + expf(-r[t])));
        y = rbf(y * sg);
        float xn = rbf(rbf(bf2f(q[t * D + d]) - mean[t]) / den[t]);
        out[t * D + d] = f2bf(xa[t * D + d] + rbf(xn + y));
    }
}
extern "C" void kernel_launch(void* const* d_in, const int* in_sizes, int n_in, void* d_out, int out_size, void* d_ws, size_t ws_size, hipStream_t stream) {
    const u16 *q = (const u16*)d_in[0], *k = (const u16*)d_in[1], *v = (const u16*)d_in[2], *sk = (const u16*)d_in[3], *h = (const u16*)d_in[4], *Dp = (const u16*)d_in[5];
    const u16 *wr = (const u16*)d_in[7], *wg = (const u16*)d_in[8], *wu = (const u16*)d_in[9], *wd = (const u16*)d_in[10];
    float* ws = (float*)d_ws; float *mean = ws, *den = ws + S, *r = ws + 2 * S, *hbuf = ws + 3 * S, *xa = ws + 8 * S;
    u16* out = (u16*)d_out;
    k_stats<<<S / 4, 256, 0, stream>>>(q, wr, mean, den, r);
    k_conv<<<D, 256, 0, stream>>>(q, k, v, sk, h, Dp, mean, den, out, xa);
    k_ffn1<<<(F + 63) / 64, 64, 0, stream>>>(q, mean, den, r, wg, wu, hbuf);
    k_ffn2<<<D / 64, 64, 0, stream>>>(q, mean, den, r, wd, hbuf, xa, out);
}
```

```cpp
#include <hip/hip_runtime.h>

typedef unsigned short u16;
typedef unsigned long long u64;
typedef __bf16 bf16x8 __attribute__((ext_vector_type(8)));
typedef __bf16 bf16x2 __attribute__((ext_vector_type(2)));
typedef short s16x4 __attribute__((ext_vector_type(4)));
typedef short s16x2 __attribute__((ext_vector_type(2)));
typedef float f32x4 __attribute__((ext_vector_type(4)));
typedef float f32x2 __attribute__((ext_vector_type(2)));
typedef unsigned u32x2 __attribute__((ext_vector_type(2)));
typedef unsigned u32x4 __attribute__((ext_vector_type(4)));
#define LAS __attribute__((address_space(3)))
#define GAS __attribute__((address_space(1)))
#define DEV __device__ __forceinline__

constexpr int SEQ = 2048, DM = 640, FF = 1728, ROWB = DM * 2;
constexpr int NFFN = 54;
constexpr unsigned WS_H1 = 4096;
constexpr unsigned WS_SEL = 40960;
constexpr unsigned WS_PART = 65536;
constexpr int LDS_BYTES = 148 * 1024;
constexpr unsigned L_X = LDS_BYTES - 1024;
constexpr unsigned L_JUNK = LDS_BYTES - 256;
constexpr int SPIN_CAP = 1 << 16;

struct Args {
    const char *q, *k, *v, *sk, *h, *dsk, *wr, *wg, *wu, *wd;
    char *out, *ws;
};

#define STAMP(i)
#define STAMP_ARG
#define STAMP_PASS

DEV void dma16(unsigned voff, const char* base, unsigned lds) {
    asm volatile("s_mov_b32 m0, %2\n\ts_nop 0\n\tglobal_load_lds_dwordx4 %0, %1" ::"v"(voff), "s"(base), "s"(lds) : "memory");
}
DEV void warm(unsigned voff, const char* base) {
    asm volatile("s_mov_b32 m0, %2\n\ts_nop 0\n\tglobal_load_lds_dword %0, %1" ::"v"(voff), "s"(base), "i"(L_JUNK) : "memory");
}
DEV u32x4 gld16(unsigned voff, const char* base) { u32x4 r; asm volatile("global_load_dwordx4 %0, %1, %2" : "=v"(r) : "v"(voff), "s"(base) : "memory"); return r; }
DEV unsigned gld4(unsigned voff, const char* base) { unsigned r; asm volatile("global_load_dword %0, %1, %2" : "=v"(r) : "v"(voff), "s"(base) : "memory"); return r; }
DEV unsigned gld2(unsigned voff, const char* base) { unsigned r; asm volatile("global_load_ushort %0, %1, %2" : "=v"(r) : "v"(voff), "s"(base) : "memory"); return r; }
DEV u32x2 gld8_dev(unsigned voff, const char* base) { u32x2 r; asm volatile("global_load_dwordx2 %0, %1, %2 sc1" : "=v"(r) : "v"(voff), "s"(base) : "memory"); return r; }
DEV void gst8_dev(unsigned voff, char* base, u32x2 x) { asm volatile("global_store_dwordx2 %0, %1, %2 sc1" ::"v"(voff), "v"(x), "s"(base) : "memory"); }
DEV void gst16(unsigned voff, char* base, u32x4 x) { asm volatile("global_store_dwordx4 %0, %1, %2\n\ts_nop 1" ::"v"(voff), "v"(x), "s"(base) : "memory"); }
#define WAITV(n) asm volatile("s_waitcnt vmcnt(%0)" ::"i"(n) : "memory")
#define PIN(x) asm volatile("" : "+v"(x))
DEV void wg_barrier() { asm volatile("s_waitcnt lgkmcnt(0)\n\ts_barrier" ::: "memory"); }

DEV u32x2 lds_r8(unsigned a) { return *(volatile LAS u32x2*)(size_t)a; }
DEV u32x4 lds_r16(unsigned a) { return *(LAS u32x4*)(size_t)a; }
DEV u32x2 lds_tr(unsigned a) { return __builtin_bit_cast(u32x2, __builtin_amdgcn_ds_read_tr16_b64_v4i16((LAS s16x4*)(size_t)a)); }
DEV void lds_w8(unsigned a, u32x2 x) { *(LAS u32x2*)(size_t)a = x; }
DEV void lds_w4(unsigned a, unsigned x) { *(LAS unsigned*)(size_t)a = x; }
DEV unsigned lds_r4(unsigned a) { return *(LAS unsigned*)(size_t)a; }
DEV u32x4 cat(u32x2 a, u32x2 b) { return (u32x4){a.x, a.y, b.x, b.y}; }
DEV f32x4 mfma(u32x4 a, u32x4 b, f32x4 c) { return __builtin_amdgcn_mfma_f32_16x16x32_bf16(__builtin_bit_cast(bf16x8, a), __builtin_bit_cast(bf16x8, b), c, 0, 0, 0); }
DEV float blo(unsigned x) { return __uint_as_float(x << 16); }
DEV float bhi(unsigned x) { return __uint_as_float(x & 0xffff0000u); }
DEV unsigned pk(float a, float b) { return __builtin_bit_cast(unsigned, __builtin_convertvector((f32x2){a, b}, bf16x2)); }
DEV float rbf(float a) { return blo(pk(a, 0.f)); }
DEV float dot2(unsigned a, unsigned b, float c) { return __builtin_amdgcn_fdot2_f32_bf16(__builtin_bit_cast(bf16x2, a), __builtin_bit_cast(bf16x2, b), c, false); }
template <int CTRL> DEV float dppf(float x) { return __builtin_bit_cast(float, __builtin_amdgcn_update_dpp(0, __builtin_bit_cast(int, x), CTRL, 0xf, 0xf, true)); }
template <int CTRL> DEV unsigned dppu(unsigned x) { return (unsigned)__builtin_amdgcn_update_dpp(0, (int)x, CTRL, 0xf, 0xf, true); }
DEV float sum16(float x) { x += dppf<0x128>(x); x += dppf<0x124>(x); x += dppf<0x122>(x); x += dppf<0x121>(x); return x; }
DEV float sum32(float x) { x = sum16(x); auto r = __builtin_amdgcn_permlane16_swap(__float_as_uint(x), __float_as_uint(x), false, false); return __uint_as_float(r[0]) + __uint_as_float(r[1]); }
DEV float fold32(float a, float b) { auto r = __builtin_amdgcn_permlane32_swap(__float_as_uint(a), __float_as_uint(b), false, false); return __uint_as_float(r[0]) + __uint_as_float(r[1]); }
DEV unsigned umax(unsigned a, unsigned b) { return a > b ? a : b; }
DEV unsigned umin(unsigned a, unsigned b) { return a < b ? a : b; }
DEV unsigned max16(unsigned x) { x = umax(x, dppu<0x128>(x)); x = umax(x, dppu<0x124>(x)); x = umax(x, dppu<0x122>(x)); x = umax(x, dppu<0x121>(x)); return x; }
DEV unsigned max64(unsigned x) {
    x = max16(x);
    auto r = __builtin_amdgcn_permlane16_swap(x, x, false, false); x = umax(r[0], r[1]);
    auto s = __builtin_amdgcn_permlane32_swap(x, x, false, false); return umax(s[0], s[1]);
}

template <int NCH, int G0> struct Conv {
    static constexpr int NT = NCH - G0, R = 256 * NCH;
    static constexpr unsigned RAWK = 64, RAWV = RAWK + R * 16, HB = RAWV + R * 16, HS = 32 + 2 * R;
    static constexpr unsigned UB = HB + 8 * HS, PS = 8 * (16 + R / 16) + 32, CHS = 4 * PS + 16;
    static constexpr unsigned YB = UB + 8 * CHS, YS = NT * 512 + 64, END = YB + 8 * YS;
    static_assert(END <= L_X, "lds");

    static DEV void issue_h(const Args& a, int grp, int wave, int lane) {
#pragma unroll
        for (int i = 0; i < NCH / 2; i++) dma16(lane * 16, a.h + (size_t)(grp * 8 + wave) * 4096 + i * 1024, HB + wave * HS + 32 + i * 1024);
    }

    static DEV void run(const Args& a, int grp, int wave, int lane, unsigned tag, int selline STAMP_ARG) {
        const int hp = wave >> 2, wv = wave & 3;
        const int c16 = lane & 15, lg = lane >> 4;
        const char* kvsrc = (hp ? a.v : a.k) + grp * 16;
        const unsigned kvoff = (wv * 64 + lane) * ROWB, kvdst = (hp ? RAWV : RAWK) + wv * 1024;
        unsigned skw = gld2((grp * 8 + (lane & 7)) * 8 + lg * 2, a.sk);
        unsigned dv = gld2(0u * lane + (grp * 8 + wave) * 2, a.dsk);
        issue_h(a, grp, wave, lane);
        dma16(kvoff, kvsrc, kvdst);
        dma16(kvoff, kvsrc + 256 * ROWB, kvdst + 4096);
        lds_w8(UB + wave * CHS + lg * PS + c16 * 8, (u32x2){0u, 0u});
        if (lane < 4) lds_w8(HB + wave * HS + lane * 8, (u32x2){0u, 0u});
        if (lane < 8 && wave == 0) lds_w8(lane * 8, (u32x2){0u, 0u});
        WAITV(2);
        PIN(skw); PIN(dv);
        STAMP(4);
        if (lane == 0) {
            LAS u16* h0 = (LAS u16*)(size_t)(HB + wave * HS + 32);
            *h0 = (u16)pk(blo(*h0) + blo(dv), 0.f);
        }
        u32x4 w1, w2;
        {
            const unsigned wsh = skw << (16 * (lane & 1));
#pragma unroll
            for (int i = 0; i < 4; i++) { w1[i] = (c16 >> 1) == i ? wsh : 0u; w2[i] = (c16 >> 1) == i + 4 ? wsh : 0u; }
        }
        const unsigned ak = RAWK + (wave * 32 + 4 * (lane & 3) + ((lane >> 2) & 3) - lg) * 16;
        const unsigned av = RAWV + (wave * 32 + 16 * ((lane >> 1) & 1) + 4 * (c16 >> 2) + lg) * 16 + (lane & 1) * 8;
        const unsigned auw = UB + (lane & 7) * CHS + lg * PS + (16 + 2 * wave + ((lane >> 3) & 1)) * 8;
        const unsigned aur = UB + wave * CHS + (16 - 8 - 1 + c16 - 2 * lg) * 8;
        const unsigned aub = UB + wave * CHS + (lane & 3) * PS + (16 - 8 - 1 + 15 - 2 * lg) * 8;
        const unsigned ah = HB + wave * HS + 32 + 2 * (32 * lg - 4 * (c16 >> 2) + 4 * (lane & 3));
        const unsigned ayw = YB + wave * YS + lane * 8;
        const unsigned ayc = YB + wave * YS + (lg < 3 ? lane + 16 : ((c16 + 1) & 15)) * 8;
        const unsigned ayr = YB + (c16 >> 2) * YS + (16 * wv + 4 * lg + (lane & 3)) * 8;
        const int frow = 16 * (lane >> 2) + 4 * wv + (lane & 3);
        const unsigned qoff = frow * ROWB + grp * 16, soff = WS_H1 + (frow >> 3) * 128 + (frow & 7) * 8;

        f32x4 acc[NT][4], accb = {0.f, 0.f, 0.f, 0.f};
        u32x4 hf[2 * NCH];
        unsigned cp0 = 0u, cp1 = 0u;
        u32x4 qx; u32x2 sx;
#pragma unroll
        for (int it = 0; it < NCH + 2; it++) {
            if (it < NCH) {
                const int nd = it + 1 < NCH ? 1 : 0;
                const bool ldA = it - 2 >= G0, stP = it - 4 >= G0, stC = it - 3 >= G0;
                const int nA = nd + (ldA ? 2 : 0) + (stP ? 1 : 0), nC = nd + (stC ? 1 : 0);
                if (nA == nC) WAITV(nA);
                else if (hp == (it & 1)) WAITV(nA);
                else WAITV(nC);
            }
            wg_barrier();
            if (it < 12) STAMP(5 + it);
            if (it - 1 >= G0 && it - 1 < NCH && hp == ((it - 1) & 1)) {
                qx = gld16(qoff, a.q + (size_t)(it - 1) * 256 * ROWB);
                sx = gld8_dev(soff, a.ws + (it - 1) * 4096);
            }
            if (it + 2 < NCH) dma16(kvoff, kvsrc + (size_t)(it + 2) * 256 * ROWB, kvdst + (it + 2) * 4096);
            if (it < NCH) {
                const u32x4 k1 = lds_r16(ak + it * 4096), k2 = lds_r16(ak + it * 4096 + 256);
                const u32x2 vv = lds_tr(av + it * 4096);
                f32x4 d = mfma(k1, w1, (f32x4){0.f, 0.f, 0.f, 0.f});
                d = mfma(k2, w2, d);
                lds_w8(auw + it * 128, (u32x2){pk(d[0] * blo(vv.x), d[1] * bhi(vv.x)), pk(d[2] * blo(vv.y), d[3] * bhi(vv.y))});
            }
            if (it >= 1 && it <= NCH) {
                const int c = it - 1;
#pragma unroll
                for (int e = 2 * c - 1; e <= 2 * c; e++) {
                    const int glo = G0 > 0 ? G0 - 1 : 0;
#pragma unroll
                    for (int g = glo; g < NCH; g++) {
                        const int w = 2 * g - e;
                        if (g >= c && (c == 0 || g == glo)) hf[w] = cat(lds_tr(ah + 32 + w * 256), lds_tr(ah + w * 256));
                    }
#pragma unroll
                    for (int ps = 0; ps < 4; ps++) {
                        const unsigned ua = aur + ps * PS + (e + 1) * 64;
                        const u32x4 uf = cat(lds_r8(ua), lds_r8(ua + 8));
#pragma unroll
                        for (int g = G0; g < NCH; g++)
                            if (g >= c) acc[g - G0][ps] = mfma(hf[2 * g - e], uf, e == -1 ? (f32x4){0.f, 0.f, 0.f, 0.f} : acc[g - G0][ps]);
                    }
                    if (G0 > 0 && c <= G0 - 1) {
                        const unsigned ua = aub + (e + 1) * 64;
                        accb = mfma(hf[2 * (G0 - 1) - e], cat(lds_r8(ua), lds_r8(ua + 8)), accb);
                    }
                }
                if (G0 > 0 && c == G0 - 1) {
                    cp0 = pk(accb[1] + dppf<0x111>(accb[2]) + dppf<0x112>(accb[3]), accb[2] + dppf<0x111>(accb[3]));
                    cp1 = pk(accb[3], 0.f);
                }
                if (c >= G0) {
                    const f32x4 d0 = acc[c - G0][0], d1 = acc[c - G0][1], d2 = acc[c - G0][2], d3 = acc[c - G0][3];
                    const float e2 = d0[2] + d2[0], e3 = d0[3] + d2[1], o2 = d1[2] + d3[0], o3 = d1[3] + d3[1];
                    lds_w8(ayw + (c - G0) * 512, (u32x2){pk(d0[0], d0[1] + d1[0]), pk(e2 + d1[1], e3 + o2)});
                    const unsigned p0 = pk(d2[2] + o3, d2[3] + d3[2]), p1 = pk(d3[3], 0.f);
                    const unsigned s0 = lane == 63 ? cp0 : p0, s1 = lane == 63 ? cp1 : p1;
                    asm volatile("ds_pk_add_bf16 %0, %1 offset:%3\n\tds_pk_add_bf16 %0, %2 offset:%4" ::"v"(ayc), "v"(s0), "v"(s1), "i"((c - G0) * 512), "i"((c - G0) * 512 + 4) : "memory");
                    cp0 = p0; cp1 = p1;
                }
            }
            if (it - 2 >= G0 && hp == (it & 1)) {
                const int g = it - 2;
                const u32x2 ya = lds_tr(ayr + (g - G0) * 512), yb = lds_tr(ayr + (g - G0) * 512 + 4 * YS);
                WAITV((it + 1 < NCH ? 1 : 0) + (it + 2 < NCH ? 1 : 0));
                PIN(qx); PIN(sx);
                _Pragma("nounroll") for (int spin = 0; spin < SPIN_CAP && __any(sx.y != tag); spin++) { sx = gld8_dev(soff, a.ws + g * 4096); WAITV(0); PIN(sx); }
                const float rs = __builtin_amdgcn_rcpf(bhi(sx.x)), c0 = -blo(sx.x) * rs;
                const u32x4 yy = cat(ya, yb);
                u32x4 o;
#pragma unroll
                for (int i = 0; i < 4; i++) {
                    const float x0 = blo(qx[i]), x1 = bhi(qx[i]), n0 = fmaf(x0, rs, c0), n1 = fmaf(x1, rs, c0);
                    o[i] = pk(fmaf(n0, blo(yy[i]), x0 + n0), fmaf(n1, bhi(yy[i]), x1 + n1));
                }
                gst16(qoff, a.out + (size_t)g * 256 * ROWB, o);
            }
        }
        STAMP(17);
        WAITV(0);
        wg_barrier();
        STAMP(18);
        if (wave < 2) {
            u32x2 sel;
            _Pragma("nounroll") for (int spin = 0; spin < SPIN_CAP; spin++) { sel = gld8_dev(0u * lane + WS_SEL + selline * 128, a.ws); WAITV(0); PIN(sel); if (__all(sel.y == tag)) break; }
            const int row = __builtin_amdgcn_readfirstlane(wave ? sel.x >> 16 : sel.x & 0xffff);
            STAMP(19);
            if (row >= 256 * G0 && row < R) {
                const int ch = lane & 7, f0 = lane >> 3;
                unsigned xq = gld2(row * ROWB + (grp * 8 + ch) * 2, a.q);
                u32x2 st, p[7];
                _Pragma("nounroll") for (int spin = 0; spin < SPIN_CAP; spin++) {
                    st = gld8_dev(0u * lane + WS_H1 + (row >> 3) * 128 + (row & 7) * 8, a.ws);
#pragma unroll
                    for (int i = 0; i < 7; i++) { const int f = i < 6 ? i * 8 + f0 : (f0 < 6 ? 48 + f0 : 53); p[i] = gld8_dev(WS_PART + ((f * 2 + wave) * DM + grp * 8 + ch) * 8, a.ws); }
                    WAITV(0);
                    PIN(st); bool ok = st.y == tag;
#pragma unroll
                    for (int i = 0; i < 7; i++) { PIN(p[i]); ok &= p[i].y == tag; }
                    if (__all(ok)) break;
                }
                PIN(xq);
                STAMP(20);
                float s = f0 < 6 ? __uint_as_float(p[6].x) : 0.f;
#pragma unroll
                for (int i = 0; i < 6; i++) s += __uint_as_float(p[i].x);
                s += dppf<0x128>(s);
                { auto r = __builtin_amdgcn_permlane16_swap(__float_as_uint(s), __float_as_uint(s), false, false); s = __uint_as_float(r[0]) + __uint_as_float(r[1]); }
                s = fold32(s, s);
                const int tr = row & 255;
                const float y = blo(*(LAS u16*)(size_t)(YB + ch * YS + ((row >> 8) - G0) * 512 + (((tr >> 2) & 3) * 16 + (tr >> 4)) * 8 + (tr & 3) * 2));
                const float rs = __builtin_amdgcn_rcpf(bhi(st.x)), x = blo(xq), n = (x - blo(st.x)) * rs;
                const unsigned o = pk(fmaf(n, y, x + n) + s, 0.f);
                if (lane < 8) *(GAS u16*)(a.out + row * ROWB + (grp * 8 + ch) * 2) = (u16)o;
                if (grp == 0 && wave == 0 && lane == 0) __hip_atomic_store((GAS unsigned*)a.ws, tag, __ATOMIC_RELAXED, __HIP_MEMORY_SCOPE_AGENT);
            }
        }
    }
};

DEV void ffn_run(const Args& a, int f, int wave, int lane, unsigned tag STAMP_ARG) {
    constexpr unsigned WGU = 0, WDN = 81920, RED = WDN + 40960, HX = RED + 4096;
    const int kq = wave & 3, mat = wave >> 2, c16 = lane & 15, lg = lane >> 4, tid = wave * 64 + lane;
    {
        const char* wsrc = (mat ? a.wu : a.wg) + (size_t)kq * 160 * FF * 2 + f * 64;
        const unsigned woff = (lane >> 2) * (FF * 2) + (lane & 3) * 16;
        const int rot = (f & 1) * 5;
#pragma unroll
        for (int i = 0; i < 5; i++) dma16(woff, wsrc + (size_t)(i + rot) * 16 * FF * 2, WGU + wave * 10240 + (i + rot) * 1024);
#pragma unroll
        for (int i = 0; i < 5; i++) dma16(lane * 16, a.wd + (size_t)f * 40960 + (wave * 5 + i) * 1024, WDN + (wave * 5 + i) * 1024);
#pragma unroll
        for (int i = 0; i < 5; i++) dma16(woff, wsrc + (size_t)(i + 5 - rot) * 16 * FF * 2, WGU + wave * 10240 + (i + 5 - rot) * 1024);
    }
    WAITV(0);
    wg_barrier();
    STAMP(4);
    u32x2 kw[4];
    const unsigned koff = WS_H1 + (tid >> 3) * 128 + 64 + (tid & 7) * 8;
#pragma unroll
    for (int i = 0; i < 4; i++) kw[i] = gld8_dev(koff + i * 8192, a.ws);
    u32x4 bg[5][2], bd[5];
    {
        const unsigned ag = WGU + wave * 10240 + (8 * lg + (c16 >> 2)) * 64 + (lane & 3) * 8;
#pragma unroll
        for (int s = 0; s < 5; s++)
#pragma unroll
            for (int n = 0; n < 2; n++) bg[s][n] = cat(lds_tr(ag + s * 2048 + n * 32), lds_tr(ag + s * 2048 + n * 32 + 256));
        const unsigned ad = WDN + (8 * lg + (c16 >> 2)) * ROWB + wave * 160 + (lane & 3) * 8;
#pragma unroll
        for (int n = 0; n < 5; n++) bd[n] = cat(lds_tr(ad + n * 32), lds_tr(ad + n * 32 + 4 * ROWB));
    }
    _Pragma("nounroll") for (int spin = 0; spin < SPIN_CAP; spin++) {
        WAITV(0);
        bool ok = true;
#pragma unroll
        for (int i = 0; i < 4; i++) { PIN(kw[i]); ok &= kw[i].y == tag; }
        if (__all(ok)) break;
#pragma unroll
        for (int i = 0; i < 4; i++) kw[i] = gld8_dev(koff + i * 8192, a.ws);
    }
    STAMP(5);
    unsigned k1, k2;
    {
        const unsigned a0 = umax(kw[0].x, kw[1].x), b0 = umin(kw[0].x, kw[1].x), a1 = umax(kw[2].x, kw[3].x), b1 = umin(kw[2].x, kw[3].x);
        const unsigned hi = umax(a0, a1), lo = umax(umin(a0, a1), umax(b0, b1));
        const unsigned m1 = max64(hi), m2 = max64(hi == m1 ? lo : hi);
        if (lane == 0) lds_w8(L_X + 128 + wave * 8, (u32x2){m1, m2});
        wg_barrier();
        const unsigned x = lds_r4(L_X + 128 + c16 * 4);
        k1 = max16(x); k2 = max16(x == k1 ? 0u : x);
    }
    const int i0 = 2047 - (k1 & 2047), i1 = 2047 - (k2 & 2047);
    u32x4 xa[5];
    const unsigned ones = c16 == 2 ? 0x3f803f80u : 0u;
#pragma unroll
    for (int s = 0; s < 5; s++) xa[s] = (u32x4){ones, ones, ones, ones};
    const int trow = (lane >> 5) ? i1 : i0;
    const unsigned stoff = WS_H1 + (trow >> 3) * 128 + (trow & 7) * 8;
    if (c16 < 2) {
        const unsigned xo = (c16 ? i1 : i0) * ROWB + kq * 320 + lg * 16;
#pragma unroll
        for (int s = 0; s < 5; s++) asm volatile("global_load_dwordx4 %0, %1, %2 offset:%3" : "+v"(xa[s]) : "v"(xo), "s"(a.q), "i"(s * 64) : "memory");
    }
    u32x2 st = gld8_dev(stoff, a.ws);
    if (tid == 0) gst8_dev(WS_SEL + f * 128, a.ws, (u32x2){(unsigned)i0 | ((unsigned)i1 << 16), tag});
    STAMP(6);
    if (wave == 0) WAITV(1); else WAITV(0);
    f32x4 g0 = {0.f, 0.f, 0.f, 0.f}, g1 = g0;
#pragma unroll
    for (int s = 0; s < 5; s++) PIN(xa[s]);
    PIN(st);
    STAMP(7);
#pragma unroll
    for (int s = 0; s < 5; s++) { g0 = mfma(xa[s], bg[s][0], g0); g1 = mfma(xa[s], bg[s][1], g1); }
    if (lg == 0) {
#pragma unroll
        for (int r = 0; r < 3; r++) { lds_w4(RED + (r * 32 + c16) * 32 + wave * 4, __float_as_uint(g0[r])); lds_w4(RED + (r * 32 + 16 + c16) * 32 + wave * 4, __float_as_uint(g1[r])); }
    }
    _Pragma("nounroll") for (int spin = 0; spin < SPIN_CAP && __any(st.y != tag); spin++) { st = gld8_dev(stoff, a.ws); WAITV(0); PIN(st); }
    wg_barrier();
    {
        const u32x4 pg = lds_r16(RED + lane * 32), pu = lds_r16(RED + lane * 32 + 16);
        const u32x4 cg = lds_r16(RED + (64 + (lane & 31)) * 32), cu = lds_r16(RED + (64 + (lane & 31)) * 32 + 16);
        auto s4 = [](u32x4 v) { return (__uint_as_float(v.x) + __uint_as_float(v.y)) + (__uint_as_float(v.z) + __uint_as_float(v.w)); };
        const float m = blo(st.x), rs = __builtin_amdgcn_rcpf(bhi(st.x));
        const float g = rbf((s4(pg) - m * s4(cg)) * rs), u = rbf((s4(pu) - m * s4(cu)) * rs);
        const float hv = rbf(g * __builtin_amdgcn_rcpf(1.0f + __expf(-g))) * u;
        *(LAS u16*)(size_t)(HX + wave * 128 + lane * 2) = (u16)pk(hv, 0.f);
    }
    const u32x4 ha = lds_r16(HX + wave * 128 + ((c16 >> 2) & 1) * 64 + lg * 16);
    const unsigned kk = (lg & 1) ? k2 : k1, rb = kk >> 11;
    const float rv = blo((rb & 0x8000u) ? (rb & 0x7fffu) : (~rb & 0xffffu));
    const float sg = rbf(__builtin_amdgcn_rcpf(1.0f + __expf(-rv)));
    float dn[5];
#pragma unroll
    for (int n = 0; n < 5; n++) dn[n] = mfma(ha, bd[n], (f32x4){0.f, 0.f, 0.f, 0.f})[0];
    const unsigned po = WS_PART + ((f * 2 + (lg & 1)) * DM + wave * 80 + (lg >> 1) * 16 + c16) * 8;
    gst8_dev(po, a.ws, (u32x2){__float_as_uint((lg < 2 ? dn[0] : dn[1]) * sg), tag});
    gst8_dev(po + 256, a.ws, (u32x2){__float_as_uint((lg < 2 ? dn[2] : dn[3]) * sg), tag});
    if (lg < 2) gst8_dev(po + 512, a.ws, (u32x2){__float_as_uint(dn[4] * sg), tag});
    STAMP(8);
}

__global__ void __launch_bounds__(512, 2) h3_mod_kernel(Args a) {
    __shared__ __attribute__((aligned(16))) char smem[LDS_BYTES];
    asm volatile("" ::"v"((LAS char*)smem));
    STAMP(0);
    const int b = blockIdx.x, x = b & 7, j = b >> 3;
    const int wave = __builtin_amdgcn_readfirstlane(threadIdx.x >> 6), lane = threadIdx.x & 63;
    unsigned tag;
    asm volatile("s_load_dword %0, %1, 0x0" : "=s"(tag) : "s"(a.ws) : "memory");
    const int nconv = x < 4 ? 24 : 16;
    const int line = j < 16 ? x : 8 + (x >> 1), late = j < 16 ? j >> 3 : x & 1;
    const int grp = line * 8 + (j & 7);
    const int nun = x >= 4 ? 5 : (x < 3 ? 2 : 1), un0 = x >= 4 ? (x - 4) * 5 : 20 + 2 * x;
    const int js = j - nconv;
    if (wave < 4) {
        const char* qr = a.q + (size_t)(b * 8 + wave * 2) * ROWB;
        u32x4 wa = gld16(lane * 16, a.wr); unsigned wb = gld4(1024 + lane * 4, a.wr);
        u32x4 xa = gld16(lane * 16, qr); unsigned xb = gld4(1024 + lane * 4, qr);
        u32x4 ya = gld16(ROWB + lane * 16, qr); unsigned yb = gld4(ROWB + 1024 + lane * 4, qr);
        STAMP(1);
        WAITV(4);
        PIN(wa); PIN(wb);
        const unsigned one = 0x3f803f80u;
        float sw = dot2(wb, one, 0.f);
#pragma unroll
        for (int i = 0; i < 4; i++) sw = dot2(wa[i], one, sw);
        sw = sum32(fold32(sw, sw));
        WAITV(0);
        PIN(xa); PIN(xb); PIN(ya); PIN(yb);
        STAMP(2);
        float s0 = dot2(xb, one, 0.f), s1 = dot2(xb, xb, 0.f), s2 = dot2(xb, wb, 0.f), t0 = dot2(yb, one, 0.f), t1 = dot2(yb, yb, 0.f), t2 = dot2(yb, wb, 0.f);
#pragma unroll
        for (int i = 0; i < 4; i++) {
            s0 = dot2(xa[i], one, s0); s1 = dot2(xa[i], xa[i], s1); s2 = dot2(xa[i], wa[i], s2);
            t0 = dot2(ya[i], one, t0); t1 = dot2(ya[i], ya[i], t1); t2 = dot2(ya[i], wa[i], t2);
        }
        s0 = sum32(fold32(s0, t0)); s1 = sum32(fold32(s1, t1)); s2 = sum32(fold32(s2, t2));
        const float mean = s0 * (1.0f / DM), var = fmaxf(fmaf(-mean, mean, s1 * (1.0f / DM)), 0.f);
        const unsigned md = pk(mean, __builtin_amdgcn_sqrtf(rbf(rbf(var) + 1e-5f)));
        const unsigned rb = pk((s2 - blo(md) * sw) * __builtin_amdgcn_rcpf(bhi(md)), 0.f);
        const int rl = wave * 2 + (lane >> 5);
        const unsigned key = (((rb & 0x8000u) ? (~rb & 0xffffu) : (rb | 0x8000u)) << 11) | (unsigned)(2047 - (b * 8 + rl));
        if ((lane & 31) == 0) { lds_w4(L_X + rl * 4, md); lds_w4(L_X + 32 + rl * 4, key); }
    } else {
        const int w4 = wave - 4;
        if (js < 0) {
            const int m = j < 16 ? j : (j & 7) * 2;
            const char* src = ((w4 & 1) ? a.v : a.k) + line * 128;
            if (j < 16) warm(((w4 >> 1) * 1024 + m * 64 + lane) * ROWB, src);
            else { warm(((w4 >> 1) * 64 + m * 64 + lane) * ROWB, src); if (late || m < 8) warm((1024 + (w4 >> 1) * 64 + m * 64 + lane) * ROWB, src); }
        } else if (js >= 2 * nun) {
            const int hi = js - 2 * nun;
            if (hi == 0 && w4 < 2) warm((w4 * 64 + lane) * 128, (const char*)(__builtin_amdgcn_s_getpc() & ~127ull));
        }
    }
    wg_barrier();
    asm volatile("" : "+s"(tag));
    tag += 0x9E3779B9u;
    if (wave == 0 && lane < 16) gst8_dev(WS_H1 + b * 128 + lane * 8, a.ws, (u32x2){lds_r4(L_X + lane * 4), tag});
    STAMP(3);
    if (js < 0) {
        const int selline = un0 * 2 + (j & 1);
        if (late) Conv<8, 6>::run(a, grp, wave, lane, tag, selline STAMP_PASS);
        else Conv<6, 0>::run(a, grp, wave, lane, tag, selline STAMP_PASS);
    } else if (js < 2 * nun) {
        ffn_run(a, un0 * 2 + js, wave, lane, tag STAMP_PASS);
    } else {
        const int hi = js - 2 * nun, nh = 32 - nconv - 2 * nun;
        for (int i = hi * 8 + wave; i < 32; i += nh * 8) warm((i * 64 + lane) * ROWB, a.q + x * 128);
        if (x < 4) { const int r0 = (x & 1) ? 24 : 0, r1 = (x & 1) ? 32 : 24; for (int i = r0 + hi * 8 + wave; i < r1; i += nh * 8) warm((i * 64 + lane) * ROWB, a.q + (8 + (x >> 1)) * 128); }
        WAITV(0);
        STAMP(4);
    }
}

extern "C" void kernel_launch(void* const* d_in, const int* in_sizes, int n_in, void* d_out, int out_size, void* d_ws, size_t ws_size, hipStream_t stream) {
    Args a;
    a.q = (const char*)d_in[0]; a.k = (const char*)d_in[1]; a.v = (const char*)d_in[2]; a.sk = (const char*)d_in[3];
    a.h = (const char*)d_in[4]; a.dsk = (const char*)d_in[5]; a.wr = (const char*)d_in[7]; a.wg = (const char*)d_in[8];
    a.wu = (const char*)d_in[9]; a.wd = (const char*)d_in[10]; a.out = (char*)d_out; a.ws = (char*)d_ws;
    h3_mod_kernel<<<256, 512, 0, stream>>>(a);
}
```

```cpp
#include <hip/hip_runtime.h>

typedef unsigned short u16;
typedef unsigned long long u64;
typedef __bf16 bf16x8 __attribute__((ext_vector_type(8)));
typedef __bf16 bf16x2 __attribute__((ext_vector_type(2)));
typedef short s16x4 __attribute__((ext_vector_type(4)));
typedef short s16x2 __attribute__((ext_vector_type(2)));
typedef float f32x4 __attribute__((ext_vector_type(4)));
typedef float f32x2 __attribute__((ext_vector_type(2)));
typedef unsigned u32x2 __attribute__((ext_vector_type(2)));
typedef unsigned u32x4 __attribute__((ext_vector_type(4)));
#define LAS __attribute__((address_space(3)))
#define GAS __attribute__((address_space(1)))
#define DEV __device__ __forceinline__

constexpr int SEQ = 2048, DM = 640, FF = 1728, ROWB = DM * 2;
constexpr int NFFN = 54;
constexpr unsigned WS_H1 = 4096;
constexpr unsigned WS_SEL = 40960;
constexpr unsigned WS_PART = 65536;
constexpr int LDS_BYTES = 148 * 1024;
constexpr unsigned L_X = LDS_BYTES - 1024;
constexpr unsigned L_JUNK = LDS_BYTES - 256;
constexpr int SPIN_CAP = 1 << 16;

struct Args {
    const char *q, *k, *v, *sk, *h, *dsk, *wr, *wg, *wu, *wd;
    char *out, *ws;
};

#define STAMP(i) if ((i) == 0 || (i) == 0) do { u64 t_; asm volatile("s_memrealtime %0\n\ts_waitcnt lgkmcnt(0)" : "=s"(t_) :: "memory"); stamps[i] = t_; } while (0)
#define STAMP_ARG , u64 (&stamps)[32]
#define STAMP_PASS , stamps

DEV void dma16(unsigned voff, const char* base, unsigned lds) {
    asm volatile("s_mov_b32 m0, %2\n\ts_nop 0\n\tglobal_load_lds_dwordx4 %0, %1 nt" ::"v"(voff), "s"(base), "s"(lds) : "memory");
}
DEV void warm(unsigned voff, const char* base) {
    asm volatile("s_mov_b32 m0, %2\n\ts_nop 0\n\tglobal_load_lds_dword %0, %1 nt" ::"v"(voff), "s"(base), "i"(L_JUNK) : "memory");
}
DEV u32x4 gld16(unsigned voff, const char* base) { u32x4 r; asm volatile("global_load_dwordx4 %0, %1, %2 nt" : "=v"(r) : "v"(voff), "s"(base) : "memory"); return r; }
DEV unsigned gld4(unsigned voff, const char* base) { unsigned r; asm volatile("global_load_dword %0, %1, %2 nt" : "=v"(r) : "v"(voff), "s"(base) : "memory"); return r; }
DEV unsigned gld2(unsigned voff, const char* base) { unsigned r; asm volatile("global_load_ushort %0, %1, %2" : "=v"(r) : "v"(voff), "s"(base) : "memory"); return r; }
DEV u32x2 gld8_dev(unsigned voff, const char* base) { u32x2 r; asm volatile("global_load_dwordx2 %0, %1, %2 sc1" : "=v"(r) : "v"(voff), "s"(base) : "memory"); return r; }
DEV void gst8_dev(unsigned voff, char* base, u32x2 x) { asm volatile("global_store_dwordx2 %0, %1, %2 sc1" ::"v"(voff), "v"(x), "s"(base) : "memory"); }
DEV void gst16(unsigned voff, char* base, u32x4 x) { asm volatile("global_store_dwordx4 %0, %1, %2 sc0 sc1\n\ts_nop 1" ::"v"(voff), "v"(x), "s"(base) : "memory"); }
#define WAITV(n) asm volatile("s_waitcnt vmcnt(%0)" ::"i"(n) : "memory")
#define PIN(x) asm volatile("" : "+v"(x))
DEV void wg_barrier() { asm volatile("s_waitcnt lgkmcnt(0)\n\ts_barrier" ::: "memory"); }

DEV u32x2 lds_r8(unsigned a) { return *(volatile LAS u32x2*)(size_t)a; }
DEV u32x4 lds_r16(unsigned a) { return *(LAS u32x4*)(size_t)a; }
DEV u32x2 lds_tr(unsigned a) { return __builtin_bit_cast(u32x2, __builtin_amdgcn_ds_read_tr16_b64_v4i16((LAS s16x4*)(size_t)a)); }
DEV void lds_w8(unsigned a, u32x2 x) { *(LAS u32x2*)(size_t)a = x; }
DEV void lds_w4(unsigned a, unsigned x) { *(LAS unsigned*)(size_t)a = x; }
DEV unsigned lds_r4(unsigned a) { return *(LAS unsigned*)(size_t)a; }
DEV u32x4 cat(u32x2 a, u32x2 b) { return (u32x4){a.x, a.y, b.x, b.y}; }
DEV f32x4 mfma(u32x4 a, u32x4 b, f32x4 c) { return __builtin_amdgcn_mfma_f32_16x16x32_bf16(__builtin_bit_cast(bf16x8, a), __builtin_bit_cast(bf16x8, b), c, 0, 0, 0); }
DEV float blo(unsigned x) { return __uint_as_float(x << 16); }
DEV float bhi(unsigned x) { return __uint_as_float(x & 0xffff0000u); }
DEV unsigned pk(float a, float b) { return __builtin_bit_cast(unsigned, __builtin_convertvector((f32x2){a, b}, bf16x2)); }
DEV float rbf(float a) { return blo(pk(a, 0.f)); }
DEV float dot2(unsigned a, unsigned b, float c) { return __builtin_amdgcn_fdot2_f32_bf16(__builtin_bit_cast(bf16x2, a), __builtin_bit_cast(bf16x2, b), c, false); }
template <int CTRL> DEV float dppf(float x) { return __builtin_bit_cast(float, __builtin_amdgcn_update_dpp(0, __builtin_bit_cast(int, x), CTRL, 0xf, 0xf, true)); }
template <int CTRL> DEV unsigned dppu(unsigned x) { return (unsigned)__builtin_amdgcn_update_dpp(0, (int)x, CTRL, 0xf, 0xf, true); }
DEV float sum16(float x) { x += dppf<0x128>(x); x += dppf<0x124>(x); x += dppf<0x122>(x); x += dppf<0x121>(x); return x; }
DEV float sum32(float x) { x = sum16(x); auto r = __builtin_amdgcn_permlane16_swap(__float_as_uint(x), __float_as_uint(x), false, false); return __uint_as_float(r[0]) + __uint_as_float(r[1]); }
DEV float fold32(float a, float b) { auto r = __builtin_amdgcn_permlane32_swap(__float_as_uint(a), __float_as_uint(b), false, false); return __uint_as_float(r[0]) + __uint_as_float(r[1]); }
DEV unsigned umax(unsigned a, unsigned b) { return a > b ? a : b; }
DEV unsigned umin(unsigned a, unsigned b) { return a < b ? a : b; }
DEV unsigned max16(unsigned x) { x = umax(x, dppu<0x128>(x)); x = umax(x, dppu<0x124>(x)); x = umax(x, dppu<0x122>(x)); x = umax(x, dppu<0x121>(x)); return x; }
DEV unsigned max64(unsigned x) {
    x = max16(x);
    auto r = __builtin_amdgcn_permlane16_swap(x, x, false, false); x = umax(r[0], r[1]);
    auto s = __builtin_amdgcn_permlane32_swap(x, x, false, false); return umax(s[0], s[1]);
}

template <int NCH, int G0> struct Conv {
    static constexpr int NT = NCH - G0, R = 256 * NCH;
    static constexpr unsigned RAWK = 64, RAWV = RAWK + R * 16, HB = RAWV + R * 16, HS = 32 + 2 * R;
    static constexpr unsigned UB = HB + 8 * HS, PS = 8 * (16 + R / 16) + 32, CHS = 4 * PS + 16;
    static constexpr unsigned YB = UB + 8 * CHS, YS = NT * 512 + 64, END = YB + 8 * YS;
    static_assert(END <= L_X, "lds");

    static DEV void issue_h(const Args& a, int grp, int wave, int lane) {
#pragma unroll
        for (int i = 0; i < NCH / 2; i++) dma16(lane * 16, a.h + (size_t)(grp * 8 + wave) * 4096 + i * 1024, HB + wave * HS + 32 + i * 1024);
    }

    static DEV void run(const Args& a, int grp, int wave, int lane, unsigned tag, int selline STAMP_ARG) {
        const int hp = wave >> 2, wv = wave & 3;
        const int c16 = lane & 15, lg = lane >> 4;
        const char* kvsrc = (hp ? a.v : a.k) + grp * 16;
        const unsigned kvoff = (wv * 64 + lane) * ROWB, kvdst = (hp ? RAWV : RAWK) + wv * 1024;
        unsigned skw = gld2((grp * 8 + (lane & 7)) * 8 + lg * 2, a.sk);
        unsigned dv = gld2(0u * lane + (grp * 8 + wave) * 2, a.dsk);
        issue_h(a, grp, wave, lane);
        dma16(kvoff, kvsrc, kvdst);
        dma16(kvoff, kvsrc + 256 * ROWB, kvdst + 4096);
        lds_w8(UB + wave * CHS + lg * PS + c16 * 8, (u32x2){0u, 0u});
        if (lane < 4) lds_w8(HB + wave * HS + lane * 8, (u32x2){0u, 0u});
        if (lane < 8 && wave == 0) lds_w8(lane * 8, (u32x2){0u, 0u});
        WAITV(2);
        PIN(skw); PIN(dv);
        STAMP(4);
        if (lane == 0) {
            LAS u16* h0 = (LAS u16*)(size_t)(HB + wave * HS + 32);
            *h0 = (u16)pk(blo(*h0) + blo(dv), 0.f);
        }
        u32x4 w1, w2;
        {
            const unsigned wsh = skw << (16 * (lane & 1));
#pragma unroll
            for (int i = 0; i < 4; i++) { w1[i] = (c16 >> 1) == i ? wsh : 0u; w2[i] = (c16 >> 1) == i + 4 ? wsh : 0u; }
        }
        const unsigned ak = RAWK + (wave * 32 + 4 * (lane & 3) + ((lane >> 2) & 3) - lg) * 16;
        const unsigned av = RAWV + (wave * 32 + 16 * ((lane >> 1) & 1) + 4 * (c16 >> 2) + lg) * 16 + (lane & 1) * 8;
        const unsigned auw = UB + (lane & 7) * CHS + lg * PS + (16 + 2 * wave + ((lane >> 3) & 1)) * 8;
        const unsigned aur = UB + wave * CHS + (16 - 8 - 1 + c16 - 2 * lg) * 8;
        const unsigned aub = UB + wave * CHS + (lane & 3) * PS + (16 - 8 - 1 + 15 - 2 * lg) * 8;
        const unsigned ah = HB + wave * HS + 32 + 2 * (32 * lg - 4 * (c16 >> 2) + 4 * (lane & 3));
        const unsigned ayw = YB + wave * YS + lane * 8;
        const unsigned ayc = YB + wave * YS + (lg < 3 ? lane + 16 : ((c16 + 1) & 15)) * 8;
        const unsigned ayr = YB + (c16 >> 2) * YS + (16 * wv + 4 * lg + (lane & 3)) * 8;
        const int frow = 16 * (lane >> 2) + 4 * wv + (lane & 3);
        const unsigned qoff = frow * ROWB + grp * 16, soff = WS_H1 + (frow >> 3) * 128 + (frow & 7) * 8;

        f32x4 acc[NT][4], accb = {0.f, 0.f, 0.f, 0.f};
        u32x4 hf[2 * NCH];
        unsigned cp0 = 0u, cp1 = 0u;
        u32x4 qx; u32x2 sx;
#pragma unroll
        for (int it = 0; it < NCH + 2; it++) {
            if (it < NCH) {
                const int nd = it + 1 < NCH ? 1 : 0;
                const bool ldA = it - 2 >= G0, stP = it - 4 >= G0, stC = it - 3 >= G0;
                const int nA = nd + (ldA ? 2 : 0) + (stP ? 1 : 0), nC = nd + (stC ? 1 : 0);
                if (nA == nC) WAITV(nA);
                else if (hp == (it & 1)) WAITV(nA);
                else WAITV(nC);
            }
            wg_barrier();
            if (it < 12) STAMP(5 + it);
            if (it - 1 >= G0 && it - 1 < NCH && hp == ((it - 1) & 1)) {
                qx = gld16(qoff, a.q + (size_t)(it - 1) * 256 * ROWB);
                sx = gld8_dev(soff, a.ws + (it - 1) * 4096);
            }
            if (it + 2 < NCH) dma16(kvoff, kvsrc + (size_t)(it + 2) * 256 * ROWB, kvdst + (it + 2) * 4096);
            if (it < NCH) {
                const u32x4 k1 = lds_r16(ak + it * 4096), k2 = lds_r16(ak + it * 4096 + 256);
                const u32x2 vv = lds_tr(av + it * 4096);
                f32x4 d = mfma(k1, w1, (f32x4){0.f, 0.f, 0.f, 0.f});
                d = mfma(k2, w2, d);
                lds_w8(auw + it * 128, (u32x2){pk(d[0] * blo(vv.x), d[1] * bhi(vv.x)), pk(d[2] * blo(vv.y), d[3] * bhi(vv.y))});
            }
            if (it >= 1 && it <= NCH) {
                const int c = it - 1;
#pragma unroll
                for (int e = 2 * c - 1; e <= 2 * c; e++) {
                    const int glo = G0 > 0 ? G0 - 1 : 0;
#pragma unroll
                    for (int g = glo; g < NCH; g++) {
                        const int w = 2 * g - e;
                        if (g >= c && (c == 0 || g == glo)) hf[w] = cat(lds_tr(ah + 32 + w * 256), lds_tr(ah + w * 256));
                    }
#pragma unroll
                    for (int ps = 0; ps < 4; ps++) {
                        const unsigned ua = aur + ps * PS + (e + 1) * 64;
                        const u32x4 uf = cat(lds_r8(ua), lds_r8(ua + 8));
#pragma unroll
                        for (int g = G0; g < NCH; g++)
                            if (g >= c) acc[g - G0][ps] = mfma(hf[2 * g - e], uf, e == -1 ? (f32x4){0.f, 0.f, 0.f, 0.f} : acc[g - G0][ps]);
                    }
                    if (G0 > 0 && c <= G0 - 1) {
                        const unsigned ua = aub + (e + 1) * 64;
                        accb = mfma(hf[2 * (G0 - 1) - e], cat(lds_r8(ua), lds_r8(ua + 8)), accb);
                    }
                }
                if (G0 > 0 && c == G0 - 1) {
                    cp0 = pk(accb[1] + dppf<0x111>(accb[2]) + dppf<0x112>(accb[3]), accb[2] + dppf<0x111>(accb[3]));
                    cp1 = pk(accb[3], 0.f);
                }
                if (c >= G0) {
                    const f32x4 d0 = acc[c - G0][0], d1 = acc[c - G0][1], d2 = acc[c - G0][2], d3 = acc[c - G0][3];
                    const float e2 = d0[2] + d2[0], e3 = d0[3] + d2[1], o2 = d1[2] + d3[0], o3 = d1[3] + d3[1];
                    lds_w8(ayw + (c - G0) * 512, (u32x2){pk(d0[0], d0[1] + d1[0]), pk(e2 + d1[1], e3 + o2)});
                    const unsigned p0 = pk(d2[2] + o3, d2[3] + d3[2]), p1 = pk(d3[3], 0.f);
                    const unsigned s0 = lane == 63 ? cp0 : p0, s1 = lane == 63 ? cp1 : p1;
                    asm volatile("ds_pk_add_bf16 %0, %1 offset:%3\n\tds_pk_add_bf16 %0, %2 offset:%4" ::"v"(ayc), "v"(s0), "v"(s1), "i"((c - G0) * 512), "i"((c - G0) * 512 + 4) : "memory");
                    cp0 = p0; cp1 = p1;
                }
            }
            if (it - 2 >= G0 && hp == (it & 1)) {
                const int g = it - 2;
                const u32x2 ya = lds_tr(ayr + (g - G0) * 512), yb = lds_tr(ayr + (g - G0) * 512 + 4 * YS);
                WAITV((it + 1 < NCH ? 1 : 0) + (it + 2 < NCH ? 1 : 0));
                PIN(qx); PIN(sx);
                _Pragma("nounroll") for (int spin = 0; spin < SPIN_CAP && __any(sx.y != tag); spin++) { sx = gld8_dev(soff, a.ws + g * 4096); WAITV(0); PIN(sx); }
                const float rs = __builtin_amdgcn_rcpf(bhi(sx.x)), c0 = -blo(sx.x) * rs;
                const u32x4 yy = cat(ya, yb);
                u32x4 o;
#pragma unroll
                for (int i = 0; i < 4; i++) {
                    const float x0 = blo(qx[i]), x1 = bhi(qx[i]), n0 = fmaf(x0, rs, c0), n1 = fmaf(x1, rs, c0);
                    o[i] = pk(fmaf(n0, blo(yy[i]), x0 + n0), fmaf(n1, bhi(yy[i]), x1 + n1));
                }
                gst16(qoff, a.out + (size_t)g * 256 * ROWB, o);
            }
        }
        STAMP(17);
        WAITV(0);
        wg_barrier();
        STAMP(18);
        if (wave < 2) {
            u32x2 sel;
            _Pragma("nounroll") for (int spin = 0; spin < SPIN_CAP; spin++) { sel = gld8_dev(0u * lane + WS_SEL + selline * 128, a.ws); WAITV(0); PIN(sel); if (__all(sel.y == tag)) break; }
            const int row = __builtin_amdgcn_readfirstlane(wave ? sel.x >> 16 : sel.x & 0xffff);
            STAMP(19);
            if (row >= 256 * G0 && row < R) {
                const int ch = lane & 7, f0 = lane >> 3;
                unsigned xq = gld2(row * ROWB + (grp * 8 + ch) * 2, a.q);
                u32x2 st, p[7];
                _Pragma("nounroll") for (int spin = 0; spin < SPIN_CAP; spin++) {
                    st = gld8_dev(0u * lane + WS_H1 + (row >> 3) * 128 + (row & 7) * 8, a.ws);
#pragma unroll
                    for (int i = 0; i < 7; i++) { const int f = i < 6 ? i * 8 + f0 : (f0 < 6 ? 48 + f0 : 53); p[i] = gld8_dev(WS_PART + ((f * 2 + wave) * DM + grp * 8 + ch) * 8, a.ws); }
                    WAITV(0);
                    PIN(st); bool ok = st.y == tag;
#pragma unroll
                    for (int i = 0; i < 7; i++) { PIN(p[i]); ok &= p[i].y == tag; }
                    if (__all(ok)) break;
                }
                PIN(xq);
                STAMP(20);
                float s = f0 < 6 ? __uint_as_float(p[6].x) : 0.f;
#pragma unroll
                for (int i = 0; i < 6; i++) s += __uint_as_float(p[i].x);
                s += dppf<0x128>(s);
                { auto r = __builtin_amdgcn_permlane16_swap(__float_as_uint(s), __float_as_uint(s), false, false); s = __uint_as_float(r[0]) + __uint_as_float(r[1]); }
                s = fold32(s, s);
                const int tr = row & 255;
                const float y = blo(*(LAS u16*)(size_t)(YB + ch * YS + ((row >> 8) - G0) * 512 + (((tr >> 2) & 3) * 16 + (tr >> 4)) * 8 + (tr & 3) * 2));
                const float rs = __builtin_amdgcn_rcpf(bhi(st.x)), x = blo(xq), n = (x - blo(st.x)) * rs;
                const unsigned o = pk(fmaf(n, y, x + n) + s, 0.f);
                if (lane < 8) *(GAS u16*)(a.out + row * ROWB + (grp * 8 + ch) * 2) = (u16)o;
                if (grp == 0 && wave == 0 && lane == 0) __hip_atomic_store((GAS unsigned*)a.ws, tag, __ATOMIC_RELAXED, __HIP_MEMORY_SCOPE_AGENT);
            }
        }
    }
};

DEV void ffn_run(const Args& a, int f, int wave, int lane, unsigned tag STAMP_ARG) {
    constexpr unsigned WGU = 0, WDN = 81920, RED = WDN + 40960, HX = RED + 4096;
    const int kq = wave & 3, mat = wave >> 2, c16 = lane & 15, lg = lane >> 4, tid = wave * 64 + lane;
    {
        const char* wsrc = (mat ? a.wu : a.wg) + (size_t)kq * 160 * FF * 2 + f * 64;
        const unsigned woff = (lane >> 2) * (FF * 2) + (lane & 3) * 16;
        const int rot = (f & 1) * 5;
#pragma unroll
        for (int i = 0; i < 5; i++) dma16(woff, wsrc + (size_t)(i + rot) * 16 * FF * 2, WGU + wave * 10240 + (i + rot) * 1024);
#pragma unroll
        for (int i = 0; i < 5; i++) dma16(lane * 16, a.wd + (size_t)f * 40960 + (wave * 5 + i) * 1024, WDN + (wave * 5 + i) * 1024);
#pragma unroll
        for (int i = 0; i < 5; i++) dma16(woff, wsrc + (size_t)(i + 5 - rot) * 16 * FF * 2, WGU + wave * 10240 + (i + 5 - rot) * 1024);
    }
    WAITV(0);
    wg_barrier();
    STAMP(4);
    u32x2 kw[4];
    const unsigned koff = WS_H1 + (tid >> 3) * 128 + 64 + (tid & 7) * 8;
#pragma unroll
    for (int i = 0; i < 4; i++) kw[i] = gld8_dev(koff + i * 8192, a.ws);
    u32x4 bg[5][2], bd[5];
    {
        const unsigned ag = WGU + wave * 10240 + (8 * lg + (c16 >> 2)) * 64 + (lane & 3) * 8;
#pragma unroll
        for (int s = 0; s < 5; s++)
#pragma unroll
            for (int n = 0; n < 2; n++) bg[s][n] = cat(lds_tr(ag + s * 2048 + n * 32), lds_tr(ag + s * 2048 + n * 32 + 256));
        const unsigned ad = WDN + (8 * lg + (c16 >> 2)) * ROWB + wave * 160 + (lane & 3) * 8;
#pragma unroll
        for (int n = 0; n < 5; n++) bd[n] = cat(lds_tr(ad + n * 32), lds_tr(ad + n * 32 + 4 * ROWB));
    }
    _Pragma("nounroll") for (int spin = 0; spin < SPIN_CAP; spin++) {
        WAITV(0);
        bool ok = true;
#pragma unroll
        for (int i = 0; i < 4; i++) { PIN(kw[i]); ok &= kw[i].y == tag; }
        if (__all(ok)) break;
#pragma unroll
        for (int i = 0; i < 4; i++) kw[i] = gld8_dev(koff + i * 8192, a.ws);
    }
    STAMP(5);
    unsigned k1, k2;
    {
        const unsigned a0 = umax(kw[0].x, kw[1].x), b0 = umin(kw[0].x, kw[1].x), a1 = umax(kw[2].x, kw[3].x), b1 = umin(kw[2].x, kw[3].x);
        const unsigned hi = umax(a0, a1), lo = umax(umin(a0, a1), umax(b0, b1));
        const unsigned m1 = max64(hi), m2 = max64(hi == m1 ? lo : hi);
        if (lane == 0) lds_w8(L_X + 128 + wave * 8, (u32x2){m1, m2});
        wg_barrier();
        const unsigned x = lds_r4(L_X + 128 + c16 * 4);
        k1 = max16(x); k2 = max16(x == k1 ? 0u : x);
    }
    const int i0 = 2047 - (k1 & 2047), i1 = 2047 - (k2 & 2047);
    u32x4 xa[5];
    const unsigned ones = c16 == 2 ? 0x3f803f80u : 0u;
#pragma unroll
    for (int s = 0; s < 5; s++) xa[s] = (u32x4){ones, ones, ones, ones};
    const int trow = (lane >> 5) ? i1 : i0;
    const unsigned stoff = WS_H1 + (trow >> 3) * 128 + (trow & 7) * 8;
    if (c16 < 2) {
        const unsigned xo = (c16 ? i1 : i0) * ROWB + kq * 320 + lg * 16;
#pragma unroll
        for (int s = 0; s < 5; s++) asm volatile("global_load_dwordx4 %0, %1, %2 offset:%3 nt" : "+v"(xa[s]) : "v"(xo), "s"(a.q), "i"(s * 64) : "memory");
    }
    u32x2 st = gld8_dev(stoff, a.ws);
    if (tid == 0) gst8_dev(WS_SEL + f * 128, a.ws, (u32x2){(unsigned)i0 | ((unsigned)i1 << 16), tag});
    STAMP(6);
    if (wave == 0) WAITV(1); else WAITV(0);
    f32x4 g0 = {0.f, 0.f, 0.f, 0.f}, g1 = g0;
#pragma unroll
    for (int s = 0; s < 5; s++) PIN(xa[s]);
    PIN(st);
    STAMP(7);
#pragma unroll
    for (int s = 0; s < 5; s++) { g0 = mfma(xa[s], bg[s][0], g0); g1 = mfma(xa[s], bg[s][1], g1); }
    if (lg == 0) {
#pragma unroll
        for (int r = 0; r < 3; r++) { lds_w4(RED + (r * 32 + c16) * 32 + wave * 4, __float_as_uint(g0[r])); lds_w4(RED + (r * 32 + 16 + c16) * 32 + wave * 4, __float_as_uint(g1[r])); }
    }
    _Pragma("nounroll") for (int spin = 0; spin < SPIN_CAP && __any(st.y != tag); spin++) { st = gld8_dev(stoff, a.ws); WAITV(0); PIN(st); }
    wg_barrier();
    {
        const u32x4 pg = lds_r16(RED + lane * 32), pu = lds_r16(RED + lane * 32 + 16);
        const u32x4 cg = lds_r16(RED + (64 + (lane & 31)) * 32), cu = lds_r16(RED + (64 + (lane & 31)) * 32 + 16);
        auto s4 = [](u32x4 v) { return (__uint_as_float(v.x) + __uint_as_float(v.y)) + (__uint_as_float(v.z) + __uint_as_float(v.w)); };
        const float m = blo(st.x), rs = __builtin_amdgcn_rcpf(bhi(st.x));
        const float g = rbf((s4(pg) - m * s4(cg)) * rs), u = rbf((s4(pu) - m * s4(cu)) * rs);
        const float hv = rbf(g * __builtin_amdgcn_rcpf(1.0f + __expf(-g))) * u;
        *(LAS u16*)(size_t)(HX + wave * 128 + lane * 2) = (u16)pk(hv, 0.f);
    }
    const u32x4 ha = lds_r16(HX + wave * 128 + ((c16 >> 2) & 1) * 64 + lg * 16);
    const unsigned kk = (lg & 1) ? k2 : k1, rb = kk >> 11;
    const float rv = blo((rb & 0x8000u) ? (rb & 0x7fffu) : (~rb & 0xffffu));
    const float sg = rbf(__builtin_amdgcn_rcpf(1.0f + __expf(-rv)));
    float dn[5];
#pragma unroll
    for (int n = 0; n < 5; n++) dn[n] = mfma(ha, bd[n], (f32x4){0.f, 0.f, 0.f, 0.f})[0];
    const unsigned po = WS_PART + ((f * 2 + (lg & 1)) * DM + wave * 80 + (lg >> 1) * 16 + c16) * 8;
    gst8_dev(po, a.ws, (u32x2){__float_as_uint((lg < 2 ? dn[0] : dn[1]) * sg), tag});
    gst8_dev(po + 256, a.ws, (u32x2){__float_as_uint((lg < 2 ? dn[2] : dn[3]) * sg), tag});
    if (lg < 2) gst8_dev(po + 512, a.ws, (u32x2){__float_as_uint(dn[4] * sg), tag});
    STAMP(8);
}

__global__ void __launch_bounds__(512, 2) h3_mod_kernel(Args a) {
    __shared__ __attribute__((aligned(16))) char smem[LDS_BYTES];
    asm volatile("" ::"v"((LAS char*)smem));
    u64 cyc0; asm volatile("s_memtime %0\n\ts_waitcnt lgkmcnt(0)" : "=s"(cyc0) :: "memory");
    u64 stamps[32];
#pragma unroll
    for (int i = 0; i < 32; i++) stamps[i] = 0;
    STAMP(0);
    const int b = blockIdx.x, x = b & 7, j = b >> 3;
    const int wave = __builtin_amdgcn_readfirstlane(threadIdx.x >> 6), lane = threadIdx.x & 63;
    unsigned tag;
    asm volatile("s_load_dword %0, %1, 0x0" : "=s"(tag) : "s"(a.ws) : "memory");
    const int nconv = x < 4 ? 24 : 16;
    const int line = j < 16 ? x : 8 + (x >> 1), late = j < 16 ? j >> 3 : x & 1;
    const int grp = line * 8 + (j & 7);
    const int nun = x >= 4 ? 5 : (x < 3 ? 2 : 1), un0 = x >= 4 ? (x - 4) * 5 : 20 + 2 * x;
    const int js = j - nconv;
    if (wave < 4) {
        const char* qr = a.q + (size_t)(b * 8 + wave * 2) * ROWB;
        u32x4 wa = gld16(lane * 16, a.wr); unsigned wb = gld4(1024 + lane * 4, a.wr);
        u32x4 xa = gld16(lane * 16, qr); unsigned xb = gld4(1024 + lane * 4, qr);
        u32x4 ya = gld16(ROWB + lane * 16, qr); unsigned yb = gld4(ROWB + 1024 + lane * 4, qr);
        STAMP(1);
        WAITV(4);
        PIN(wa); PIN(wb);
        const unsigned one = 0x3f803f80u;
        float sw = dot2(wb, one, 0.f);
#pragma unroll
        for (int i = 0; i < 4; i++) sw = dot2(wa[i], one, sw);
        sw = sum32(fold32(sw, sw));
        WAITV(0);
        PIN(xa); PIN(xb); PIN(ya); PIN(yb);
        STAMP(2);
        float s0 = dot2(xb, one, 0.f), s1 = dot2(xb, xb, 0.f), s2 = dot2(xb, wb, 0.f), t0 = dot2(yb, one, 0.f), t1 = dot2(yb, yb, 0.f), t2 = dot2(yb, wb, 0.f);
#pragma unroll
        for (int i = 0; i < 4; i++) {
            s0 = dot2(xa[i], one, s0); s1 = dot2(xa[i], xa[i], s1); s2 = dot2(xa[i], wa[i], s2);
            t0 = dot2(ya[i], one, t0); t1 = dot2(ya[i], ya[i], t1); t2 = dot2(ya[i], wa[i], t2);
        }
        s0 = sum32(fold32(s0, t0)); s1 = sum32(fold32(s1, t1)); s2 = sum32(fold32(s2, t2));
        const float mean = s0 * (1.0f / DM), var = fmaxf(fmaf(-mean, mean, s1 * (1.0f / DM)), 0.f);
        const unsigned md = pk(mean, __builtin_amdgcn_sqrtf(rbf(rbf(var) + 1e-5f)));
        const unsigned rb = pk((s2 - blo(md) * sw) * __builtin_amdgcn_rcpf(bhi(md)), 0.f);
        const int rl = wave * 2 + (lane >> 5);
        const unsigned key = (((rb & 0x8000u) ? (~rb & 0xffffu) : (rb | 0x8000u)) << 11) | (unsigned)(2047 - (b * 8 + rl));
        if ((lane & 31) == 0) { lds_w4(L_X + rl * 4, md); lds_w4(L_X + 32 + rl * 4, key); }
    } else {
        const int w4 = wave - 4;
        if (js < 0) {
            const int m = j < 16 ? j : (j & 7) * 2;
            const char* src = ((w4 & 1) ? a.v : a.k) + line * 128;
            if (j < 16) warm(((w4 >> 1) * 1024 + m * 64 + lane) * ROWB, src);
            else { warm(((w4 >> 1) * 64 + m * 64 + lane) * ROWB, src); if (late || m < 8) warm((1024 + (w4 >> 1) * 64 + m * 64 + lane) * ROWB, src); }
        } else if (js >= 2 * nun) {
            const int hi = js - 2 * nun;
            if (hi == 0 && w4 < 2) warm((w4 * 64 + lane) * 128, (const char*)(__builtin_amdgcn_s_getpc() & ~127ull));
        }
    }
    wg_barrier();
    asm volatile("" : "+s"(tag));
    tag += 0x9E3779B9u;
    if (wave == 0 && lane < 16) gst8_dev(WS_H1 + b * 128 + lane * 8, a.ws, (u32x2){lds_r4(L_X + lane * 4), tag});
    STAMP(3);
    if (js < 0) {
        const int selline = un0 * 2 + (j & 1);
        if (late) Conv<8, 6>::run(a, grp, wave, lane, tag, selline STAMP_PASS);
        else Conv<6, 0>::run(a, grp, wave, lane, tag, selline STAMP_PASS);
    } else if (js < 2 * nun) {
        ffn_run(a, un0 * 2 + js, wave, lane, tag STAMP_PASS);
    } else {
        const int hi = js - 2 * nun, nh = 32 - nconv - 2 * nun;
        for (int i = hi * 8 + wave; i < 32; i += nh * 8) warm((i * 64 + lane) * ROWB, a.q + x * 128);
        if (x < 4) { const int r0 = (x & 1) ? 24 : 0, r1 = (x & 1) ? 32 : 24; for (int i = r0 + hi * 8 + wave; i < r1; i += nh * 8) warm((i * 64 + lane) * ROWB, a.q + (8 + (x >> 1)) * 128); }
        WAITV(0);
        STAMP(4);
    }
    WAITV(0);
    STAMP(31);
    {
        auto rt = []() { u64 t_; asm volatile("s_memrealtime %0\n\ts_waitcnt lgkmcnt(0)" : "=s"(t_) :: "memory"); return t_; };
        u64 end = stamps[0] + 4000;
        const int role = js < 0 ? (late ? 1 : 0) : (js < 2 * nun ? 2 : 3);

        while (rt() < end) __builtin_amdgcn_s_sleep(2);
    }
}

extern "C" void kernel_launch(void* const* d_in, const int* in_sizes, int n_in, void* d_out, int out_size, void* d_ws, size_t ws_size, hipStream_t stream) {
    Args a;
    a.q = (const char*)d_in[0]; a.k = (const char*)d_in[1]; a.v = (const char*)d_in[2]; a.sk = (const char*)d_in[3];
    a.h = (const char*)d_in[4]; a.dsk = (const char*)d_in[5]; a.wr = (const char*)d_in[7]; a.wg = (const char*)d_in[8];
    a.wu = (const char*)d_in[9]; a.wd = (const char*)d_in[10]; a.out = (char*)d_out; a.ws = (char*)d_ws;
    h3_mod_kernel<<<256, 512, 0, stream>>>(a);
}
```

```cpp
#include <hip/hip_runtime.h>

struct Args {
    const void *k, *v, *q, *wr, *sk;
    void* ws;
    const void *h, *dsk, *wg, *wu, *wd;
    void* out;
};

__global__ void __launch_bounds__(512, 2) h3_mod_kernel(Args a) {
    __shared__ __attribute__((aligned(16))) char smem[159744];
    asm volatile(
        ".Lentry_h3:\n"
        "s_load_dwordx16 s[4:19], s[0:1], 0x0\n"
        "s_load_dwordx8 s[20:27], s[0:1], 0x40\n"
        "v_readfirstlane_b32 s28, v0\n"
        "v_and_b32 v1, 63, v0\n"
        "s_and_b32 s29, s2, 7\n"
        "s_lshr_b32 s30, s2, 3\n"
        "s_lshr_b32 s28, s28, 6\n"
        "s_lshl_b32 s52, s29, 2\n"
        "s_lshr_b32 s35, 0x8888cccc, s52\n"
        "s_and_b32 s35, s35, 15\n"
        "s_lshl_b32 s53, s29, 3\n"
        "s_mov_b32 s54, 0x24180c00\n"
        "s_mov_b32 s55, 0x48403830\n"
        "s_lshr_b64 s[54:55], s[54:55], s53\n"
        "s_and_b32 s88, s54, 0xff\n"
        "s_lshl_b32 s56, s35, 1\n"
        "s_cmp_ge_u32 s30, s56\n"
        "s_cbranch_scc0 .Lconv_h3\n"
        "s_getpc_b64 s[58:59]\n"
        "s_sub_u32 s53, 31, s30\n"
        "s_lshl3_add_u32 s53, s53, s28\n"
        "s_mul_i32 s53, s53, 3\n"
        "v_add_u32 v5, s53, v1\n"
        "v_min_u32 v5, (.Lend_h3-.Lconv_h3)/128, v5\n"
        "v_lshlrev_b32 v5, 7, v5\n"
        "s_mov_b32 m0, 159488\n"
        "s_mov_b64 exec, 7\n"
        "global_load_lds_dword v5, s[58:59]\n"
        "s_mov_b64 exec, -1\n"
        "s_branch .Lspare_h3\n"
        ".Lconv_h3:\n"
        "s_lshl1_add_u32 s52, s88, s30\n"
        "s_and_b32 s33, s52, 15\n"
        "s_lshr_b32 s53, s52, 4\n"
        "v_lshrrev_b32 v20, 3, v1\n"
        "s_lshl_b32 s54, s28, 2\n"
        "v_lshrrev_b32 v21, 1, v20\n"
        "v_add3_u32 v22, v21, s54, 2\n"
        "v_xor_b32 v22, v22, v1\n"
        "v_and_b32 v22, 7, v22\n"
        "v_mul_u32_u24 v48, 1280, v20\n"
        "v_lshl_add_u32 v48, v22, 4, v48\n"
        "s_lshl_b32 s55, s33, 3\n"
        "s_add_u32 s55, s55, s28\n"
        "s_mul_i32 s55, s55, 10240\n"
        "s_lshl_b32 s56, s53, 7\n"
        "s_add_u32 s55, s55, s56\n"
        "s_lshl_b32 s58, s28, 10\n"
        "s_add_u32 m0, s58, 512\n"
        "s_lshl_b32 s57, s53, 3\n"
        "s_add_u32 s57, s57, s28\n"
        "s_waitcnt lgkmcnt(0)\n"
        "s_add_u32 s36, s4, s55\n"
        "s_addc_u32 s37, s5, 0\n"
        "s_add_u32 s38, s6, s55\n"
        "s_addc_u32 s39, s7, 0\n"
        "global_load_lds_dwordx4 v48, s[36:37]\n"
        "s_add_u32 m0, s58, 8704\n"
        "s_nop 0\n"
        "global_load_lds_dwordx4 v48, s[38:39]\n"
        "s_cmp_gt_u32 s28, 1\n"
        "s_cbranch_scc1 .Lnohalo_h3\n"
        "v_add_u32 v21, 1, v20\n"
        "v_lshrrev_b32 v22, 1, v21\n"
        "v_xor_b32 v22, v22, v1\n"
        "v_and_b32 v22, 7, v22\n"
        "v_mul_u32_u24 v21, 1280, v20\n"
        "v_lshl_add_u32 v21, v22, 4, v21\n"
        "s_mul_i32 s59, s28, 1300480\n"
        "s_mul_i32 s60, s28, 16896\n"
        "s_add_u32 m0, s60, 128\n"
        "s_mov_b64 exec, 0xffffff\n"
        "s_add_u32 s61, s33, s28\n"
        "s_cmp_eq_u32 s61, 0\n"
        "s_cbranch_scc1 .Lzhalo_h3\n"
        "s_add_u32 s42, s36, s59\n"
        "s_addc_u32 s43, s37, 0\n"
        "s_sub_u32 s42, s42, 3840\n"
        "s_subb_u32 s43, s43, 0\n"
        "global_load_lds_dwordx4 v21, s[42:43]\n"
        "s_branch .Lhalox_h3\n"
        ".Lzhalo_h3:\n"
        "v_mov_b32 v24, 0\n"
        "v_mov_b32 v25, 0\n"
        "v_mov_b32 v26, 0\n"
        "v_mov_b32 v27, 0\n"
        "v_lshlrev_b32 v21, 4, v1\n"
        "ds_write_b128 v21, v[24:27] offset:128\n"
        ".Lhalox_h3:\n"
        "s_mov_b64 exec, -1\n"
        ".Lnohalo_h3:\n"
        "v_and_b32 v46, 15, v1\n"
        "v_lshrrev_b32 v47, 4, v1\n"
        "v_and_b32 v20, 7, v1\n"
        "v_lshlrev_b32 v20, 3, v20\n"
        "v_lshl_add_u32 v20, v47, 1, v20\n"
        "s_lshl_b32 s52, s57, 6\n"
        "s_add_u32 s42, s12, s52\n"
        "s_addc_u32 s43, s13, 0\n"
        "global_load_ushort v23, v20, s[42:43]\n"
        "s_add_u32 s36, s36, 1310720\n"
        "s_addc_u32 s37, s37, 0\n"
        "s_add_u32 s38, s38, 1310720\n"
        "s_addc_u32 s39, s39, 0\n"
        "s_load_dword s31, s[14:15], 0x0\n"
        "s_lshr_b32 s52, s29, 1\n"
        "s_add_u32 s52, s52, 8\n"
        "s_lshr_b32 s53, s30, 3\n"
        "s_cmp_lt_u32 s30, 16\n"
        "s_cselect_b32 s52, s29, s52\n"
        "s_cselect_b32 s3, s53, s29\n"
        "s_and_b32 s3, s3, 1\n"
        "s_and_b32 s53, s30, 7\n"
        "s_lshl3_add_u32 s87, s52, s53\n"
        "s_lshl_b32 s62, s87, 3\n"
        "s_add_u32 s62, s62, s28\n"
        "s_lshl_b32 s63, s62, 12\n"
        "s_mul_i32 s50, s28, 4320\n"
        "s_add_u32 s50, s50, 43232\n"
        "v_lshlrev_b32 v2, 4, v1\n"
        "s_add_u32 s40, s16, s63\n"
        "s_addc_u32 s41, s17, 0\n"
        "s_cmp_lg_u32 s28, 7\n"
        "s_cbranch_scc1 .Lnotouch_h3\n"
        "s_lshl_b32 s52, s87, 8\n"
        "s_add_u32 s52, s52, 1048576\n"
        "s_cmp_lg_u32 s3, 0\n"
        "s_cselect_b32 s53, 128, 0\n"
        "s_add_u32 s52, s52, s53\n"
        "v_mov_b32 v24, s52\n"
        "v_mov_b32 v25, 0\n"
        "s_mov_b64 exec, 1\n"
        "global_atomic_or v24, v25, s[14:15] sc1\n"
        "s_mov_b64 exec, -1\n"
        ".Lnotouch_h3:\n"
        "s_mul_i32 s54, s57, 33792\n"
        "s_lshl_b32 s55, s33, 7\n"
        "s_add_u32 s54, s54, s55\n"
        "s_add_u32 s54, s54, 2097152\n"
        "s_lshl_b32 s56, s57, 8\n"
        "s_lshl_b32 s55, s33, 2\n"
        "s_add_u32 s56, s56, s55\n"
        "s_add_u32 s56, s56, 1048576\n"
        "s_add_u32 s70, s14, s54\n"
        "s_addc_u32 s71, s15, 0\n"
        "s_add_u32 s72, s14, s56\n"
        "s_addc_u32 s73, s15, 0\n"
        "v_and_b32 v20, 3, v1\n"
        "v_lshrrev_b32 v21, 2, v46\n"
        "v_lshl_add_u32 v22, v21, 3, v20\n"
        "v_sub_u32 v22, v22, v47\n"
        "v_add_u32 v24, 4, v22\n"
        "v_bfe_u32 v25, v24, 1, 3\n"
        "v_xor_b32 v25, s28, v25\n"
        "v_lshlrev_b32 v25, 4, v25\n"
        "v_lshl_add_u32 v49, v24, 7, v25\n"
        "v_add_u32 v24, 8, v22\n"
        "v_bfe_u32 v25, v24, 1, 3\n"
        "v_xor_b32 v25, s28, v25\n"
        "v_lshlrev_b32 v25, 4, v25\n"
        "v_lshl_add_u32 v50, v24, 7, v25\n"
        "v_bfe_u32 v22, v1, 1, 1\n"
        "v_lshl_add_u32 v22, v22, 2, v47\n"
        "v_lshl_add_u32 v22, v22, 3, v21\n"
        "v_and_b32 v26, 1, v1\n"
        "v_add_u32 v24, 4, v22\n"
        "v_bfe_u32 v25, v24, 1, 3\n"
        "v_xor_b32 v25, s28, v25\n"
        "v_lshl_add_u32 v25, v25, 1, v26\n"
        "v_lshlrev_b32 v25, 3, v25\n"
        "v_lshl_add_u32 v51, v24, 7, v25\n"
        "v_add_u32 v24, 8, v22\n"
        "v_bfe_u32 v25, v24, 1, 3\n"
        "v_xor_b32 v25, s28, v25\n"
        "v_lshl_add_u32 v25, v25, 1, v26\n"
        "v_lshlrev_b32 v25, 3, v25\n"
        "v_lshl_add_u32 v52, v24, 7, v25\n"
        "v_and_b32 v22, 7, v1\n"
        "v_bfe_u32 v24, v1, 3, 1\n"
        "v_mul_u32_u24 v25, 9, v22\n"
        "v_lshl_add_u32 v25, v24, 2, v25\n"
        "v_add_u32 v25, v25, v47\n"
        "s_mul_i32 s54, s28, 1152\n"
        "v_lshl_add_u32 v53, v25, 4, s54\n"
        "v_lshrrev_b32 v24, 3, v1\n"
        "v_add_u32 v18, v1, v24\n"
        "v_lshl_add_u32 v18, v18, 4, s54\n"
        "v_mul_u32_u24 v25, 4224, v24\n"
        "v_lshl_add_u32 v54, v22, 4, v25\n"
        "v_lshlrev_b32 v26, 4, v26\n"
        "v_lshrrev_b32 v19, 1, v46\n"
        "v_cmp_eq_u32 s[74:75], 0, v19\n"
        "v_cmp_eq_u32 s[76:77], 1, v19\n"
        "v_cmp_eq_u32 s[78:79], 2, v19\n"
        "v_cmp_eq_u32 s[80:81], 3, v19\n"
        "v_cmp_eq_u32 s[82:83], 4, v19\n"
        "v_cmp_eq_u32 s[84:85], 5, v19\n"
        "v_cmp_eq_u32 s[64:65], 6, v19\n"
        "v_cmp_eq_u32 s[60:61], 7, v19\n"
        "s_add_u32 m0, s58, 17408\n"
        "s_nop 0\n"
        "global_load_lds_dwordx4 v48, s[36:37]\n"
        "s_add_u32 m0, s58, 25600\n"
        "s_nop 0\n"
        "global_load_lds_dwordx4 v48, s[38:39]\n"
        "s_waitcnt lgkmcnt(0)\n"
        "s_add_u32 s31, s31, 0x9E3779B9\n"
        "s_waitcnt vmcnt(2)\n"
        "v_lshlrev_b32 v23, v26, v23\n"
        "v_cndmask_b32 v56, 0, v23, s[74:75]\n"
        "v_cndmask_b32 v57, 0, v23, s[76:77]\n"
        "v_cndmask_b32 v58, 0, v23, s[78:79]\n"
        "v_cndmask_b32 v59, 0, v23, s[80:81]\n"
        "v_cndmask_b32 v60, 0, v23, s[82:83]\n"
        "v_cndmask_b32 v61, 0, v23, s[84:85]\n"
        "v_cndmask_b32 v62, 0, v23, s[64:65]\n"
        "v_cndmask_b32 v63, 0, v23, s[60:61]\n"
        "s_barrier\n"
        "ds_read_b128 v[64:67], v49 offset:0\n"
        "ds_read_b128 v[68:71], v49 offset:4096\n"
        "ds_read_b128 v[72:75], v50 offset:0\n"
        "ds_read_b128 v[76:79], v50 offset:4096\n"
        "ds_read_b64_tr_b16 v[80:81], v51 offset:8192\n"
        "ds_read_b64_tr_b16 v[82:83], v52 offset:8192\n"
        "s_waitcnt lgkmcnt(5)\n"
        "v_mfma_f32_16x16x32_bf16 v[84:87], v[64:67], v[56:59], 0\n"
        "s_waitcnt lgkmcnt(4)\n"
        "v_mfma_f32_16x16x32_bf16 v[84:87], v[68:71], v[60:63], v[84:87]\n"
        "s_waitcnt lgkmcnt(3)\n"
        "v_mfma_f32_16x16x32_bf16 v[88:91], v[72:75], v[56:59], 0\n"
        "s_waitcnt lgkmcnt(2)\n"
        "v_mfma_f32_16x16x32_bf16 v[88:91], v[76:79], v[60:63], v[88:91]\n"
        "s_waitcnt lgkmcnt(1)\n"
        "v_lshlrev_b32 v64, 16, v80\n"
        "v_and_b32 v65, 0xffff0000, v80\n"
        "v_lshlrev_b32 v66, 16, v81\n"
        "v_and_b32 v67, 0xffff0000, v81\n"
        "s_waitcnt lgkmcnt(0)\n"
        "v_lshlrev_b32 v68, 16, v82\n"
        "v_and_b32 v69, 0xffff0000, v82\n"
        "v_lshlrev_b32 v70, 16, v83\n"
        "v_and_b32 v71, 0xffff0000, v83\n"
        "v_mul_f32 v64, v64, v84\n"
        "v_mul_f32 v65, v65, v85\n"
        "v_cvt_pk_bf16_f32 v80, v64, v65\n"
        "v_mul_f32 v66, v66, v86\n"
        "v_mul_f32 v67, v67, v87\n"
        "v_cvt_pk_bf16_f32 v81, v66, v67\n"
        "v_mul_f32 v68, v68, v88\n"
        "v_mul_f32 v69, v69, v89\n"
        "v_cvt_pk_bf16_f32 v82, v68, v69\n"
        "v_mul_f32 v70, v70, v90\n"
        "v_mul_f32 v71, v71, v91\n"
        "v_cvt_pk_bf16_f32 v83, v70, v71\n"
        "ds_write_b128 v53, v[80:83] offset:33792\n"
        "ds_read_b128 v[84:87], v18 offset:33792\n"
        "s_waitcnt lgkmcnt(0)\n"
        "global_store_dwordx4 v54, v[84:87], s[70:71] offset:0 sc1\n"
        "s_barrier\n"
        "s_cmp_lg_u32 s3, 0\n"
        "s_cselect_b32 s52, 0, 1\n"
        "s_lshl_b32 s52, s52, 10\n"
        "s_add_u32 s42, s40, s52\n"
        "s_addc_u32 s43, s41, 0\n"
        "s_add_u32 m0, s50, s52\n"
        "s_nop 0\n"
        "global_load_lds_dwordx4 v2, s[42:43]\n"
        "s_cmp_lg_u32 s3, 0\n"
        "s_cselect_b32 s52, 3, 2\n"
        "s_lshl_b32 s52, s52, 10\n"
        "s_add_u32 s42, s40, s52\n"
        "s_addc_u32 s43, s41, 0\n"
        "s_add_u32 m0, s50, s52\n"
        "s_nop 0\n"
        "global_load_lds_dwordx4 v2, s[42:43]\n"
        "s_mul_i32 s51, s28, 4128\n"
        "s_add_u32 s51, s51, 77600\n"
        "s_lshl_b32 s53, s62, 1\n"
        "v_mov_b32 v25, s53\n"
        "global_load_ushort v10, v25, s[18:19]\n"
        "s_lshl_b32 s53, s87, 4\n"
        "s_mul_i32 s54, s62, 4224\n"
        "s_add_u32 s54, s54, 2097152\n"
        "s_lshl_b32 s55, s87, 8\n"
        "s_add_u32 s55, s55, 1048576\n"
        "s_add_u32 s44, s8, s53\n"
        "s_addc_u32 s45, s9, 0\n"
        "s_add_u32 s46, s26, s53\n"
        "s_addc_u32 s47, s27, 0\n"
        "s_add_u32 s48, s14, 4096\n"
        "s_addc_u32 s49, s15, 0\n"
        "s_add_u32 s66, s14, s54\n"
        "s_addc_u32 s67, s15, 0\n"
        "s_add_u32 s68, s14, s55\n"
        "s_addc_u32 s69, s15, 0\n"
        "v_lshl_add_u32 v25, v20, 2, v21\n"
        "v_lshlrev_b32 v24, 3, v47\n"
        "v_sub_u32 v25, v25, v24\n"
        "s_sub_u32 s54, s50, 32\n"
        "v_lshl_add_u32 v3, v25, 3, s54\n"
        "v_sub_u32 v25, v20, v21\n"
        "v_add_u32 v25, v25, v24\n"
        "v_lshl_add_u32 v4, v25, 3, s51\n"
        "s_lshl_b32 s54, s28, 12\n"
        "s_lshr_b32 s55, s28, 2\n"
        "s_lshl_b32 s55, s55, 6\n"
        "s_add_u32 s54, s54, s55\n"
        "s_add_u32 s54, s54, 110592\n"
        "v_lshl_add_u32 v5, v1, 2, s54\n"
        "v_mul_u32_u24 v24, 288, v20\n"
        "v_sub_u32 v233, v3, v24\n"
        "v_add_u32 v233, 96, v233\n"
        "v_mul_u32_u24 v24, 248, v20\n"
        "v_add3_u32 v234, v4, v24, 24\n"
        "s_lshl_b32 s54, s28, 5\n"
        "v_lshrrev_b32 v25, 1, v1\n"
        "v_add_u32 v25, s54, v25\n"
        "v_and_b32 v24, 1, v1\n"
        "v_mul_u32_u24 v6, 16448, v24\n"
        "v_lshl_add_u32 v6, v25, 2, v6\n"
        "v_add_u32 v6, 110592, v6\n"
        "v_mul_u32_u24 v7, 1280, v25\n"
        "v_lshl_add_u32 v7, v24, 3, v7\n"
        "s_mul_i32 s54, s28, 1152\n"
        "s_add_u32 s54, s54, 33792\n"
        "v_lshrrev_b32 v24, 1, v1\n"
        "v_lshl_add_u32 v235, v24, 3, s54\n"
        "v_lshl_add_u32 v250, v1, 4, s54\n"
        "s_lshl_b32 s54, s28, 8\n"
        "v_lshl_add_u32 v8, v46, 4, s54\n"
        "v_lshlrev_b32 v9, 2, v1\n"
        "v_mov_b32 v12, 1.0\n"
        "v_mov_b32 v13, 0\n"
        "v_mov_b32 v14, 0\n"
        "v_mov_b32 v15, 0\n"
        "s_sub_u32 s54, s50, 224\n"
        "v_lshl_add_u32 v24, v1, 3, s54\n"
        "s_sub_u32 s54, s51, 256\n"
        "v_lshl_add_u32 v25, v1, 3, s54\n"
        "v_cmp_gt_u32 vcc, 28, v1\n"
        "s_nop 1\n"
        "v_cndmask_b32 v24, v25, v24, vcc\n"
        "s_mov_b32 exec_hi, 0\n"
        "ds_write_b64 v24, v[14:15]\n"
        "s_mov_b64 exec, -1\n"
        "v_mov_b32 v226, s31\n"
        "v_mov_b32 v227, s31\n"
        "s_waitcnt vmcnt(3)\n"
        "s_mov_b64 exec, 1\n"
        "global_atomic_swap v9, v226, s[72:73] offset:0 sc1\n"
        "s_mov_b64 exec, -1\n"
        "s_waitcnt lgkmcnt(0)\n"
        "s_barrier\n"
        "ds_read_b128 v[64:67], v49 offset:16896\n"
        "ds_read_b128 v[68:71], v49 offset:20992\n"
        "ds_read_b128 v[72:75], v50 offset:16896\n"
        "ds_read_b128 v[76:79], v50 offset:20992\n"
        "ds_read_b64_tr_b16 v[80:81], v51 offset:25088\n"
        "ds_read_b64_tr_b16 v[82:83], v52 offset:25088\n"
        "s_waitcnt lgkmcnt(5)\n"
        "v_mfma_f32_16x16x32_bf16 v[84:87], v[64:67], v[56:59], 0\n"
        "s_waitcnt lgkmcnt(4)\n"
        "v_mfma_f32_16x16x32_bf16 v[84:87], v[68:71], v[60:63], v[84:87]\n"
        "s_waitcnt lgkmcnt(3)\n"
        "v_mfma_f32_16x16x32_bf16 v[88:91], v[72:75], v[56:59], 0\n"
        "s_waitcnt lgkmcnt(2)\n"
        "v_mfma_f32_16x16x32_bf16 v[88:91], v[76:79], v[60:63], v[88:91]\n"
        "s_waitcnt lgkmcnt(1)\n"
        "v_lshlrev_b32 v64, 16, v80\n"
        "v_and_b32 v65, 0xffff0000, v80\n"
        "v_lshlrev_b32 v66, 16, v81\n"
        "v_and_b32 v67, 0xffff0000, v81\n"
        "s_waitcnt lgkmcnt(0)\n"
        "v_lshlrev_b32 v68, 16, v82\n"
        "v_and_b32 v69, 0xffff0000, v82\n"
        "v_lshlrev_b32 v70, 16, v83\n"
        "v_and_b32 v71, 0xffff0000, v83\n"
        "v_mul_f32 v64, v64, v84\n"
        "v_mul_f32 v65, v65, v85\n"
        "v_cvt_pk_bf16_f32 v80, v64, v65\n"
        "v_mul_f32 v66, v66, v86\n"
        "v_mul_f32 v67, v67, v87\n"
        "v_cvt_pk_bf16_f32 v81, v66, v67\n"
        "v_mul_f32 v68, v68, v88\n"
        "v_mul_f32 v69, v69, v89\n"
        "v_cvt_pk_bf16_f32 v82, v68, v69\n"
        "v_mul_f32 v70, v70, v90\n"
        "v_mul_f32 v71, v71, v91\n"
        "v_cvt_pk_bf16_f32 v83, v70, v71\n"
        "ds_write_b128 v53, v[80:83] offset:33792\n"
        "ds_read_b128 v[84:87], v18 offset:33792\n"
        "s_waitcnt lgkmcnt(0)\n"
        "global_store_dwordx4 v54, v[84:87], s[70:71] offset:2048 sc1\n"
        "s_cmp_lg_u32 s3, 0\n"
        "s_cselect_b32 s52, 2, 0\n"
        "s_lshl_b32 s52, s52, 10\n"
        "s_add_u32 s42, s40, s52\n"
        "s_addc_u32 s43, s41, 0\n"
        "s_add_u32 m0, s50, s52\n"
        "s_nop 0\n"
        "global_load_lds_dwordx4 v2, s[42:43]\n"
        "s_cmp_lg_u32 s3, 0\n"
        "s_cbranch_scc1 .LconvL_h3\n"
        "v_and_b32 v232, 3, v1\n"
        "v_lshrrev_b32 v24, 1, v232\n"
        "v_lshlrev_b32 v24, 6, v24\n"
        "v_lshl_add_u32 v251, v232, 13, v24\n"
        "v_add_u32 v251, 110592, v251\n"
        "v_lshrrev_b32 v24, 2, v1\n"
        "v_cmp_gt_u32 s[80:81], 6, v24\n"
        "s_lshl_b32 s54, s87, 6\n"
        "s_add_u32 s54, s54, 65536\n"
        "v_lshl_add_u32 v25, v232, 4, s54\n"
        "s_movk_i32 s54, 10240\n"
        "v_mad_u32_u24 v228, v24, s54, v25\n"
        "v_add_u32 v229, 163840, v228\n"
        "v_add_u32 v230, 327680, v228\n"
        "v_min_u32 v24, 5, v24\n"
        "v_mad_u32_u24 v231, v24, s54, v25\n"
        "v_add_u32 v231, 491520, v231\n"
        "s_lshl_b32 s53, s29, 3\n"
        "s_mov_b32 s54, 0x6040200\n"
        "s_mov_b32 s55, 0x17120d08\n"
        "s_lshr_b64 s[54:55], s[54:55], s53\n"
        "s_and_b32 s89, s54, 0xff\n"
        "s_lshl_b32 s52, s89, 8\n"
        "s_add_u32 s52, s52, 40960\n"
        "s_add_u32 s78, s14, s52\n"
        "s_addc_u32 s79, s15, 0\n"
        "v_lshrrev_b32 v24, 4, v1\n"
        "v_lshlrev_b32 v24, 2, v24\n"
        "s_mov_b32 s52, 0x5432\n"
        "v_bfe_u32 v24, s52, v24, 4\n"
        "v_lshl_add_u32 v8, v24, 11, v8\n"
        "s_mov_b32 s76, 0x1010000\n"
        "s_mov_b32 s77, 0x101\n"
        "s_lshl_b64 s[76:77], s[76:77], s28\n"
        "s_mov_b32 s74, 0x321000\n"
        "s_cmp_eq_u32 s28, 0\n"
        "s_cbranch_scc1 .LconvE_p0_h3\n"
        "s_waitcnt vmcnt(5)\n"
        "ds_read_b64_tr_b16 v[138:139], v3 offset:1280\n"
        "ds_read_b64_tr_b16 v[140:141], v3 offset:1312\n"
        "ds_read_b64_tr_b16 v[142:143], v3 offset:1408\n"
        "ds_read_b64_tr_b16 v[144:145], v3 offset:1440\n"
        "ds_read_b64_tr_b16 v[146:147], v3 offset:1536\n"
        "ds_read_b64_tr_b16 v[148:149], v3 offset:1568\n"
        "ds_read_b64_tr_b16 v[150:151], v3 offset:1664\n"
        "ds_read_b64_tr_b16 v[152:153], v3 offset:1696\n"
        "ds_read_b64_tr_b16 v[154:155], v3 offset:1792\n"
        "ds_read_b64_tr_b16 v[156:157], v3 offset:1824\n"
        "ds_read_b64_tr_b16 v[158:159], v3 offset:1920\n"
        "ds_read_b64_tr_b16 v[160:161], v3 offset:1952\n"
        "s_waitcnt vmcnt(4)\n"
        "ds_read_b64_tr_b16 v[162:163], v3 offset:2048\n"
        "ds_read_b64_tr_b16 v[164:165], v3 offset:2080\n"
        "ds_read_b64_tr_b16 v[166:167], v3 offset:2176\n"
        "ds_read_b64_tr_b16 v[168:169], v3 offset:2208\n"
        "ds_read_b64_tr_b16 v[170:171], v3 offset:2304\n"
        "ds_read_b64_tr_b16 v[172:173], v3 offset:2336\n"
        "ds_read_b64_tr_b16 v[174:175], v3 offset:2432\n"
        "ds_read_b64_tr_b16 v[176:177], v3 offset:2464\n"
        "ds_read_b64_tr_b16 v[178:179], v3 offset:2560\n"
        "ds_read_b64_tr_b16 v[180:181], v3 offset:2592\n"
        "ds_read_b64_tr_b16 v[182:183], v3 offset:2688\n"
        "ds_read_b64_tr_b16 v[184:185], v3 offset:2720\n"
        "ds_read_b64_tr_b16 v[186:187], v3 offset:2816\n"
        "ds_read_b64_tr_b16 v[188:189], v3 offset:2848\n"
        "ds_read_b64_tr_b16 v[190:191], v3 offset:2944\n"
        "ds_read_b64_tr_b16 v[192:193], v3 offset:2976\n"
        "s_branch .LconvE_np_h3\n"
        ".LconvE_p0_h3:\n"
        "s_mov_b64 exec, 65535\n"
        "s_mov_b32 s86, 32768\n"
        "global_atomic_or v236, v9, v13, s[68:69] sc0 sc1\n"
        "s_sleep 5\n"
        "global_atomic_or v237, v9, v13, s[68:69] sc0 sc1\n"
        "s_sleep 5\n"
        "global_atomic_or v238, v9, v13, s[68:69] sc0 sc1\n"
        ".LconvE_pl_h3:\n"
        "s_waitcnt vmcnt(2)\n"
        "v_cmp_ne_u32 vcc, s31, v236\n"
        "s_nop 0\n"
        "s_cbranch_vccz .LconvE_px_h3\n"
        "global_atomic_or v236, v9, v13, s[68:69] sc0 sc1\n"
        "s_waitcnt vmcnt(2)\n"
        "v_cmp_ne_u32 vcc, s31, v237\n"
        "s_nop 0\n"
        "s_cbranch_vccz .LconvE_px_h3\n"
        "global_atomic_or v237, v9, v13, s[68:69] sc0 sc1\n"
        "s_waitcnt vmcnt(2)\n"
        "v_cmp_ne_u32 vcc, s31, v238\n"
        "s_nop 0\n"
        "s_cbranch_vccz .LconvE_px_h3\n"
        "global_atomic_or v238, v9, v13, s[68:69] sc0 sc1\n"
        "s_sub_u32 s86, s86, 1\n"
        "s_cmp_lg_u32 s86, 0\n"
        "s_cbranch_scc1 .LconvE_pl_h3\n"
        ".LconvE_px_h3:\n"
        "s_mov_b64 exec, -1\n"
        ".LconvE_np_h3:\n"
        "s_barrier\n"
        "s_mov_b32 m0, s51\n"
        "s_nop 0\n"
        "global_load_lds_dwordx4 v2, s[66:67] offset:0 sc1\n"
        "s_cmp_lg_u32 s28, 0\n"
        "s_cbranch_scc1 .LconvE_n2_h3\n"
        "ds_read_b64_tr_b16 v[138:139], v3 offset:1280\n"
        "ds_read_b64_tr_b16 v[140:141], v3 offset:1312\n"
        "ds_read_b64_tr_b16 v[142:143], v3 offset:1408\n"
        "ds_read_b64_tr_b16 v[144:145], v3 offset:1440\n"
        "ds_read_b64_tr_b16 v[146:147], v3 offset:1536\n"
        "ds_read_b64_tr_b16 v[148:149], v3 offset:1568\n"
        "ds_read_b64_tr_b16 v[150:151], v3 offset:1664\n"
        "ds_read_b64_tr_b16 v[152:153], v3 offset:1696\n"
        "ds_read_b64_tr_b16 v[154:155], v3 offset:1792\n"
        "ds_read_b64_tr_b16 v[156:157], v3 offset:1824\n"
        "ds_read_b64_tr_b16 v[158:159], v3 offset:1920\n"
        "ds_read_b64_tr_b16 v[160:161], v3 offset:1952\n"
        "ds_read_b64_tr_b16 v[162:163], v3 offset:2048\n"
        "ds_read_b64_tr_b16 v[164:165], v3 offset:2080\n"
        "ds_read_b64_tr_b16 v[166:167], v3 offset:2176\n"
        "ds_read_b64_tr_b16 v[168:169], v3 offset:2208\n"
        "ds_read_b64_tr_b16 v[170:171], v3 offset:2304\n"
        "ds_read_b64_tr_b16 v[172:173], v3 offset:2336\n"
        "ds_read_b64_tr_b16 v[174:175], v3 offset:2432\n"
        "ds_read_b64_tr_b16 v[176:177], v3 offset:2464\n"
        "ds_read_b64_tr_b16 v[178:179], v3 offset:2560\n"
        "ds_read_b64_tr_b16 v[180:181], v3 offset:2592\n"
        "ds_read_b64_tr_b16 v[182:183], v3 offset:2688\n"
        "ds_read_b64_tr_b16 v[184:185], v3 offset:2720\n"
        "ds_read_b64_tr_b16 v[186:187], v3 offset:2816\n"
        "ds_read_b64_tr_b16 v[188:189], v3 offset:2848\n"
        "ds_read_b64_tr_b16 v[190:191], v3 offset:2944\n"
        "ds_read_b64_tr_b16 v[192:193], v3 offset:2976\n"
        ".LconvE_n2_h3:\n"
        "s_waitcnt vmcnt(2)\n"
        "s_mov_b64 exec, 1\n"
        "global_atomic_swap v9, v226, s[72:73] offset:128 sc1\n"
        "s_mov_b64 exec, -1\n"
        "s_waitcnt vmcnt(2)\n"
        "v_mov_b32 v38, s50\n"
        "s_mov_b64 exec, 1\n"
        "ds_read_u16 v39, v38\n"
        "v_lshlrev_b32 v10, 16, v10\n"
        "s_waitcnt lgkmcnt(0)\n"
        "v_lshlrev_b32 v39, 16, v39\n"
        "v_add_f32 v10, v10, v39\n"
        "v_cvt_pk_bf16_f32 v10, v10, v10\n"
        "ds_write_b16 v38, v10\n"
        "s_mov_b64 exec, -1\n"
        "s_mov_b32 m0, s51\n"
        "s_nop 0\n"
        "global_load_lds_dwordx4 v2, s[66:67] offset:1024 sc1\n"
        "ds_read_b64_tr_b16 v[126:127], v3 offset:896\n"
        "ds_read_b64_tr_b16 v[128:129], v3 offset:928\n"
        "ds_read_b64_tr_b16 v[130:131], v3 offset:1024\n"
        "ds_read_b64_tr_b16 v[132:133], v3 offset:1056\n"
        "ds_read_b64_tr_b16 v[134:135], v3 offset:1152\n"
        "ds_read_b64_tr_b16 v[136:137], v3 offset:1184\n"
        "ds_read_b64_tr_b16 v[118:119], v3 offset:640\n"
        "ds_read_b64_tr_b16 v[120:121], v3 offset:672\n"
        "ds_read_b64_tr_b16 v[122:123], v3 offset:768\n"
        "ds_read_b64_tr_b16 v[124:125], v3 offset:800\n"
        "ds_read_b64_tr_b16 v[110:111], v3 offset:384\n"
        "ds_read_b64_tr_b16 v[112:113], v3 offset:416\n"
        "ds_read_b64_tr_b16 v[114:115], v3 offset:512\n"
        "ds_read_b64_tr_b16 v[116:117], v3 offset:544\n"
        "ds_read_b64_tr_b16 v[102:103], v3 offset:128\n"
        "ds_read_b64_tr_b16 v[104:105], v3 offset:160\n"
        "ds_read_b64_tr_b16 v[106:107], v3 offset:256\n"
        "ds_read_b64_tr_b16 v[108:109], v3 offset:288\n"
        "ds_read_b64_tr_b16 v[98:99], v3 offset:0\n"
        "ds_read_b64_tr_b16 v[100:101], v3 offset:32\n"
        "s_waitcnt vmcnt(2)\n"
        "ds_read_b64_tr_b16 v[46:47], v4 offset:32\n"
        "ds_read_b64_tr_b16 v[48:49], v4 offset:0\n"
        "ds_read_b64_tr_b16 v[50:51], v4 offset:288\n"
        "ds_read_b64_tr_b16 v[52:53], v4 offset:256\n"
        "s_setprio 3\n"
        "s_waitcnt lgkmcnt(2)\n"
        "v_mfma_f32_16x16x32_bf16 v[94:97], v[126:129], v[46:49], 0\n"
        "s_add_u32 s42, s44, 655360\n"
        "s_addc_u32 s43, s45, 0\n"
        "global_load_dwordx2 v[236:237], v7, s[42:43]\n"
        "v_mfma_f32_16x16x32_bf16 v[62:65], v[130:133], v[46:49], v[12:15]\n"
        "v_mfma_f32_16x16x32_bf16 v[66:69], v[134:137], v[46:49], v[12:15]\n"
        "v_mfma_f32_16x16x32_bf16 v[70:73], v[138:141], v[46:49], v[12:15]\n"
        "v_mfma_f32_16x16x32_bf16 v[74:77], v[142:145], v[46:49], v[12:15]\n"
        "v_mfma_f32_16x16x32_bf16 v[78:81], v[146:149], v[46:49], v[12:15]\n"
        "v_mfma_f32_16x16x32_bf16 v[82:85], v[150:153], v[46:49], v[12:15]\n"
        "v_mfma_f32_16x16x32_bf16 v[86:89], v[154:157], v[46:49], v[12:15]\n"
        "v_mfma_f32_16x16x32_bf16 v[90:93], v[158:161], v[46:49], v[12:15]\n"
        "ds_read_b64_tr_b16 v[54:55], v4 offset:544\n"
        "s_waitcnt lgkmcnt(1)\n"
        "v_mfma_f32_16x16x32_bf16 v[94:97], v[118:121], v[50:53], v[94:97]\n"
        "ds_read_b64_tr_b16 v[56:57], v4 offset:512\n"
        "v_mfma_f32_16x16x32_bf16 v[62:65], v[122:125], v[50:53], v[62:65]\n"
        "v_mfma_f32_16x16x32_bf16 v[66:69], v[126:129], v[50:53], v[66:69]\n"
        "v_mfma_f32_16x16x32_bf16 v[70:73], v[130:133], v[50:53], v[70:73]\n"
        "v_mfma_f32_16x16x32_bf16 v[74:77], v[134:137], v[50:53], v[74:77]\n"
        "ds_read_b64_tr_b16 v[58:59], v4 offset:800\n"
        "v_mfma_f32_16x16x32_bf16 v[78:81], v[138:141], v[50:53], v[78:81]\n"
        "ds_read_b64_tr_b16 v[60:61], v4 offset:768\n"
        "v_mfma_f32_16x16x32_bf16 v[82:85], v[142:145], v[50:53], v[82:85]\n"
        "v_mfma_f32_16x16x32_bf16 v[86:89], v[146:149], v[50:53], v[86:89]\n"
        "v_mfma_f32_16x16x32_bf16 v[90:93], v[150:153], v[50:53], v[90:93]\n"
        "s_mov_b64 exec, 0xff\n"
        "global_atomic_or v227, v9, v13, s[68:69] offset:128 sc0 sc1\n"
        "s_mov_b64 exec, -1\n"
        "s_waitcnt lgkmcnt(2)\n"
        "v_mfma_f32_16x16x32_bf16 v[94:97], v[110:113], v[54:57], v[94:97]\n"
        "s_waitcnt vmcnt(2)\n"
        "ds_read_b64_tr_b16 v[46:47], v4 offset:1056\n"
        "v_mfma_f32_16x16x32_bf16 v[62:65], v[114:117], v[54:57], v[62:65]\n"
        "ds_read_b64_tr_b16 v[48:49], v4 offset:1024\n"
        "v_mfma_f32_16x16x32_bf16 v[66:69], v[118:121], v[54:57], v[66:69]\n"
        "v_mfma_f32_16x16x32_bf16 v[70:73], v[122:125], v[54:57], v[70:73]\n"
        "v_mfma_f32_16x16x32_bf16 v[74:77], v[126:129], v[54:57], v[74:77]\n"
        "ds_read_b64_tr_b16 v[50:51], v4 offset:1312\n"
        "global_load_dwordx4 v[244:247], v8, s[48:49] sc1\n"
        "s_waitcnt lgkmcnt(3)\n"
        "v_mfma_f32_16x16x32_bf16 v[94:97], v[102:105], v[58:61], v[94:97]\n"
        "ds_read_b64_tr_b16 v[52:53], v4 offset:1280\n"
        "v_mfma_f32_16x16x32_bf16 v[62:65], v[106:109], v[58:61], v[62:65]\n"
        "ds_read_b64_tr_b16 v[54:55], v4 offset:544\n"
        "s_add_u32 s42, s44, 983040\n"
        "s_addc_u32 s43, s45, 0\n"
        "global_load_dwordx2 v[238:239], v7, s[42:43]\n"
        "v_mfma_f32_16x16x32_bf16 v[66:69], v[110:113], v[58:61], v[66:69]\n"
        "ds_read_b64_tr_b16 v[56:57], v4 offset:512\n"
        "v_mfma_f32_16x16x32_bf16 v[70:73], v[114:117], v[58:61], v[70:73]\n"
        "v_mfma_f32_16x16x32_bf16 v[74:77], v[118:121], v[58:61], v[74:77]\n"
        "s_waitcnt lgkmcnt(4)\n"
        "v_mfma_f32_16x16x32_bf16 v[62:65], v[98:101], v[46:49], v[62:65]\n"
        "ds_read_b64_tr_b16 v[58:59], v4 offset:800\n"
        "v_mfma_f32_16x16x32_bf16 v[66:69], v[102:105], v[46:49], v[66:69]\n"
        "ds_read_b64_tr_b16 v[60:61], v4 offset:768\n"
        "v_mfma_f32_16x16x32_bf16 v[70:73], v[106:109], v[46:49], v[70:73]\n"
        "v_mfma_f32_16x16x32_bf16 v[74:77], v[110:113], v[46:49], v[74:77]\n"
        "s_waitcnt lgkmcnt(4)\n"
        "v_mfma_f32_16x16x32_bf16 v[70:73], v[98:101], v[50:53], v[70:73]\n"
        "ds_read_b64_tr_b16 v[46:47], v4 offset:1056\n"
        "v_mfma_f32_16x16x32_bf16 v[74:77], v[102:105], v[50:53], v[74:77]\n"
        "ds_read_b64_tr_b16 v[48:49], v4 offset:1024\n"
        "s_waitcnt lgkmcnt(4)\n"
        "v_mfma_f32_16x16x32_bf16 v[78:81], v[130:133], v[54:57], v[78:81]\n"
        "v_mfma_f32_16x16x32_bf16 v[82:85], v[134:137], v[54:57], v[82:85]\n"
        "v_add_f32_dpp v96, v97, v96 wave_shr:1 row_mask:0xf bank_mask:0xf bound_ctrl:1\n"
        "s_nop 2\n"
        "v_mov_b32 v19, v77\n"
        "v_mfma_f32_16x16x32_bf16 v[86:89], v[138:141], v[54:57], v[86:89]\n"
        "ds_read_b64_tr_b16 v[50:51], v4 offset:1312\n"
        "v_add_f32_dpp v22, v97, v64 wave_ror:1 row_mask:0xf bank_mask:0xf\n"
        "v_add_f32_dpp v23, v65, v68 wave_ror:1 row_mask:0xf bank_mask:0xf\n"
        "v_mfma_f32_16x16x32_bf16 v[90:93], v[142:145], v[54:57], v[90:93]\n"
        "ds_read_b64_tr_b16 v[52:53], v4 offset:1280\n"
        "v_add_f32_dpp v24, v69, v72 wave_ror:1 row_mask:0xf bank_mask:0xf\n"
        "v_add_f32_dpp v20, v73, v76 wave_ror:1 row_mask:0xf bank_mask:0xf\n"
        "s_waitcnt lgkmcnt(4)\n"
        "v_mfma_f32_16x16x32_bf16 v[78:81], v[122:125], v[58:61], v[78:81]\n"
        "v_add_f32_dpp v95, v96, v95 wave_shr:1 row_mask:0xf bank_mask:0xf bound_ctrl:1\n"
        "v_add_f32_dpp v22, v65, v64 wave_shr:1 row_mask:0xf bank_mask:0xf\n"
        "v_mfma_f32_16x16x32_bf16 v[82:85], v[126:129], v[58:61], v[82:85]\n"
        "v_add_f32_dpp v23, v69, v68 wave_shr:1 row_mask:0xf bank_mask:0xf\n"
        "v_add_f32_dpp v24, v73, v72 wave_shr:1 row_mask:0xf bank_mask:0xf\n"
        "v_mfma_f32_16x16x32_bf16 v[86:89], v[130:133], v[58:61], v[86:89]\n"
        "ds_read_b64_tr_b16 v[54:55], v4 offset:1568\n"
        "v_add_f32_dpp v20, v77, v76 wave_shr:1 row_mask:0xf bank_mask:0xf\n"
        "v_add_f32_dpp v64, v96, v63 wave_ror:1 row_mask:0xf bank_mask:0xf\n"
        "v_mfma_f32_16x16x32_bf16 v[90:93], v[134:137], v[58:61], v[90:93]\n"
        "ds_read_b64_tr_b16 v[56:57], v4 offset:1536\n"
        "v_add_f32_dpp v68, v22, v67 wave_ror:1 row_mask:0xf bank_mask:0xf\n"
        "v_add_f32_dpp v72, v23, v71 wave_ror:1 row_mask:0xf bank_mask:0xf\n"
        "s_waitcnt lgkmcnt(4)\n"
        "v_mfma_f32_16x16x32_bf16 v[78:81], v[114:117], v[46:49], v[78:81]\n"
        "v_add_f32_dpp v21, v24, v75 wave_ror:1 row_mask:0xf bank_mask:0xf\n"
        "v_add_f32_dpp v64, v22, v63 wave_shr:1 row_mask:0xf bank_mask:0xf\n"
        "v_mfma_f32_16x16x32_bf16 v[82:85], v[118:121], v[46:49], v[82:85]\n"
        "v_add_f32_dpp v68, v23, v67 wave_shr:1 row_mask:0xf bank_mask:0xf\n"
        "v_add_f32_dpp v72, v24, v71 wave_shr:1 row_mask:0xf bank_mask:0xf\n"
        "v_mfma_f32_16x16x32_bf16 v[86:89], v[122:125], v[46:49], v[86:89]\n"
        "s_setprio 2\n"
        "ds_read_b64_tr_b16 v[58:59], v4 offset:1824\n"
        "v_add_f32_dpp v21, v20, v75 wave_shr:1 row_mask:0xf bank_mask:0xf\n"
        "v_add_f32_dpp v63, v95, v62 wave_ror:1 row_mask:0xf bank_mask:0xf\n"
        "v_mfma_f32_16x16x32_bf16 v[90:93], v[126:129], v[46:49], v[90:93]\n"
        "ds_read_b64_tr_b16 v[60:61], v4 offset:1792\n"
        "v_add_f32_dpp v67, v64, v66 wave_ror:1 row_mask:0xf bank_mask:0xf\n"
        "v_add_f32_dpp v71, v68, v70 wave_ror:1 row_mask:0xf bank_mask:0xf\n"
        "s_waitcnt lgkmcnt(4)\n"
        "v_mfma_f32_16x16x32_bf16 v[78:81], v[106:109], v[50:53], v[78:81]\n"
        "ds_read_b64_tr_b16 v[46:47], v4 offset:32\n"
        "v_add_f32_dpp v75, v72, v74 wave_ror:1 row_mask:0xf bank_mask:0xf\n"
        "v_add_f32_dpp v63, v64, v62 wave_shr:1 row_mask:0xf bank_mask:0xf\n"
        "v_mfma_f32_16x16x32_bf16 v[82:85], v[110:113], v[50:53], v[82:85]\n"
        "ds_read_b64_tr_b16 v[48:49], v4 offset:0\n"
        "v_add_f32_dpp v67, v68, v66 wave_shr:1 row_mask:0xf bank_mask:0xf\n"
        "ds_write2st64_b32 v5, v63, v67 offset0:0 offset1:1\n"
        "v_add_f32_dpp v71, v72, v70 wave_shr:1 row_mask:0xf bank_mask:0xf\n"
        "v_mfma_f32_16x16x32_bf16 v[86:89], v[114:117], v[50:53], v[86:89]\n"
        "v_add_f32_dpp v75, v21, v74 wave_shr:1 row_mask:0xf bank_mask:0xf\n"
        "ds_write2st64_b32 v5, v71, v75 offset0:2 offset1:3\n"
        "v_mfma_f32_16x16x32_bf16 v[90:93], v[118:121], v[50:53], v[90:93]\n"
        "s_waitcnt vmcnt(2)\n"
        "v_cmp_ne_u32 vcc, s31, v227\n"
        "s_nop 0\n"
        "s_cbranch_vccnz .LconvEf1_p_h3\n"
        ".LconvEf1_r_h3:\n"
        "s_waitcnt lgkmcnt(6)\n"
        "v_mfma_f32_16x16x32_bf16 v[78:81], v[98:101], v[54:57], v[78:81]\n"
        "ds_read_b64_tr_b16 v[50:51], v4 offset:288\n"
        "s_mov_b32 m0, s51\n"
        "s_nop 0\n"
        "global_load_lds_dwordx4 v2, s[66:67] offset:2048 sc1\n"
        "v_mfma_f32_16x16x32_bf16 v[82:85], v[102:105], v[54:57], v[82:85]\n"
        "ds_read_b64_tr_b16 v[52:53], v4 offset:256\n"
        "s_waitcnt vmcnt(2)\n"
        "v_cmp_ne_u32 vcc, s31, v245\n"
        "v_cmp_ne_u32 s[84:85], s31, v247\n"
        "s_nop 0\n"
        "s_or_b64 vcc, vcc, s[84:85]\n"
        "s_cbranch_vccnz .LconvEst_p_h3\n"
        ".LconvEst_r_h3:\n"
        "v_mfma_f32_16x16x32_bf16 v[86:89], v[106:109], v[54:57], v[86:89]\n"
        "v_lshlrev_b32 v245, 16, v244\n"
        "v_and_b32 v244, 0xffff0000, v244\n"
        "v_lshlrev_b32 v247, 16, v246\n"
        "v_and_b32 v246, 0xffff0000, v246\n"
        "v_rcp_f32 v244, v244\n"
        "v_mfma_f32_16x16x32_bf16 v[90:93], v[110:113], v[54:57], v[90:93]\n"
        "v_rcp_f32 v246, v246\n"
        "v_mul_f32_e64 v245, -v245, v244\n"
        "v_mul_f32_e64 v247, -v247, v246\n"
        "ds_write_b128 v250, v[244:247]\n"
        "s_waitcnt lgkmcnt(7)\n"
        "v_mfma_f32_16x16x32_bf16 v[86:89], v[98:101], v[58:61], v[86:89]\n"
        "s_setprio 1\n"
        "ds_read_b64_tr_b16 v[54:55], v4 offset:544\n"
        "s_add_u32 s42, s44, 1310720\n"
        "s_addc_u32 s43, s45, 0\n"
        "global_load_dwordx2 v[240:241], v7, s[42:43]\n"
        "v_mfma_f32_16x16x32_bf16 v[90:93], v[102:105], v[58:61], v[90:93]\n"
        "ds_read_b64_tr_b16 v[56:57], v4 offset:512\n"
        "s_waitcnt lgkmcnt(7)\n"
        "v_mfma_f32_16x16x32_bf16 v[62:65], v[162:165], v[46:49], v[12:15]\n"
        "v_mfma_f32_16x16x32_bf16 v[66:69], v[166:169], v[46:49], v[12:15]\n"
        "s_nop 3\n"
        "v_mov_b32 v16, v93\n"
        "v_add_f32_dpp v22, v19, v80 wave_ror:1 row_mask:0xf bank_mask:0xf\n"
        "v_mfma_f32_16x16x32_bf16 v[70:73], v[170:173], v[46:49], v[12:15]\n"
        "ds_read_b64_tr_b16 v[58:59], v4 offset:800\n"
        "v_add_f32_dpp v23, v81, v84 wave_ror:1 row_mask:0xf bank_mask:0xf\n"
        "v_add_f32_dpp v24, v85, v88 wave_ror:1 row_mask:0xf bank_mask:0xf\n"
        "v_mfma_f32_16x16x32_bf16 v[74:77], v[174:177], v[46:49], v[12:15]\n"
        "ds_read_b64_tr_b16 v[60:61], v4 offset:768\n"
        "v_add_f32_dpp v17, v89, v92 wave_ror:1 row_mask:0xf bank_mask:0xf\n"
        "v_add_f32_dpp v22, v81, v80 wave_shr:1 row_mask:0xf bank_mask:0xf\n"
        "s_waitcnt lgkmcnt(5)\n"
        "v_mfma_f32_16x16x32_bf16 v[62:65], v[154:157], v[50:53], v[62:65]\n"
        "v_add_f32_dpp v23, v85, v84 wave_shr:1 row_mask:0xf bank_mask:0xf\n"
        "v_add_f32_dpp v24, v89, v88 wave_shr:1 row_mask:0xf bank_mask:0xf\n"
        "v_mfma_f32_16x16x32_bf16 v[66:69], v[158:161], v[50:53], v[66:69]\n"
        "v_add_f32_dpp v17, v93, v92 wave_shr:1 row_mask:0xf bank_mask:0xf\n"
        "v_add_f32_dpp v80, v20, v79 wave_ror:1 row_mask:0xf bank_mask:0xf\n"
        "v_mfma_f32_16x16x32_bf16 v[70:73], v[162:165], v[50:53], v[70:73]\n"
        "ds_read_b64_tr_b16 v[46:47], v4 offset:1056\n"
        "v_add_f32_dpp v84, v22, v83 wave_ror:1 row_mask:0xf bank_mask:0xf\n"
        "v_add_f32_dpp v88, v23, v87 wave_ror:1 row_mask:0xf bank_mask:0xf\n"
        "v_mfma_f32_16x16x32_bf16 v[74:77], v[166:169], v[50:53], v[74:77]\n"
        "ds_read_b64_tr_b16 v[48:49], v4 offset:1024\n"
        "v_add_f32_dpp v18, v24, v91 wave_ror:1 row_mask:0xf bank_mask:0xf\n"
        "v_add_f32_dpp v80, v22, v79 wave_shr:1 row_mask:0xf bank_mask:0xf\n"
        "s_waitcnt lgkmcnt(4)\n"
        "v_mfma_f32_16x16x32_bf16 v[62:65], v[146:149], v[54:57], v[62:65]\n"
        "v_add_f32_dpp v84, v23, v83 wave_shr:1 row_mask:0xf bank_mask:0xf\n"
        "v_add_f32_dpp v88, v24, v87 wave_shr:1 row_mask:0xf bank_mask:0xf\n"
        "v_mfma_f32_16x16x32_bf16 v[66:69], v[150:153], v[54:57], v[66:69]\n"
        "v_add_f32_dpp v18, v17, v91 wave_shr:1 row_mask:0xf bank_mask:0xf\n"
        "v_add_f32_dpp v79, v21, v78 wave_ror:1 row_mask:0xf bank_mask:0xf\n"
        "v_mfma_f32_16x16x32_bf16 v[70:73], v[154:157], v[54:57], v[70:73]\n"
        "s_setprio 0\n"
        "ds_read_b64_tr_b16 v[50:51], v4 offset:1312\n"
        "v_add_f32_dpp v83, v80, v82 wave_ror:1 row_mask:0xf bank_mask:0xf\n"
        "v_add_f32_dpp v87, v84, v86 wave_ror:1 row_mask:0xf bank_mask:0xf\n"
        "v_mfma_f32_16x16x32_bf16 v[74:77], v[158:161], v[54:57], v[74:77]\n"
        "ds_read_b64_tr_b16 v[52:53], v4 offset:1280\n"
        "v_add_f32_dpp v91, v88, v90 wave_ror:1 row_mask:0xf bank_mask:0xf\n"
        "v_add_f32_dpp v79, v80, v78 wave_shr:1 row_mask:0xf bank_mask:0xf\n"
        "s_waitcnt lgkmcnt(4)\n"
        "v_mfma_f32_16x16x32_bf16 v[62:65], v[138:141], v[58:61], v[62:65]\n"
        "v_add_f32_dpp v83, v84, v82 wave_shr:1 row_mask:0xf bank_mask:0xf\n"
        "ds_write2st64_b32 v5, v79, v83 offset0:4 offset1:5\n"
        "v_add_f32_dpp v87, v88, v86 wave_shr:1 row_mask:0xf bank_mask:0xf\n"
        "v_mfma_f32_16x16x32_bf16 v[66:69], v[142:145], v[58:61], v[66:69]\n"
        "v_add_f32_dpp v91, v18, v90 wave_shr:1 row_mask:0xf bank_mask:0xf\n"
        "ds_write2st64_b32 v5, v87, v91 offset0:6 offset1:7\n"
        "v_mfma_f32_16x16x32_bf16 v[70:73], v[146:149], v[58:61], v[70:73]\n"
        "ds_read_b64_tr_b16 v[54:55], v4 offset:1568\n"
        "v_mfma_f32_16x16x32_bf16 v[74:77], v[150:153], v[58:61], v[74:77]\n"
        "ds_read_b64_tr_b16 v[56:57], v4 offset:1536\n"
        "s_add_u32 s42, s44, 1638400\n"
        "s_addc_u32 s43, s45, 0\n"
        "global_load_dwordx2 v[242:243], v7, s[42:43]\n"
        "s_waitcnt lgkmcnt(6)\n"
        "v_mfma_f32_16x16x32_bf16 v[62:65], v[130:133], v[46:49], v[62:65]\n"
        "v_mfma_f32_16x16x32_bf16 v[66:69], v[134:137], v[46:49], v[66:69]\n"
        "s_waitcnt lgkmcnt(2)\n"
        "s_barrier\n"
        "s_setprio 3\n"
        "v_mfma_f32_16x16x32_bf16 v[70:73], v[138:141], v[46:49], v[70:73]\n"
        "ds_read_b64_tr_b16 v[58:59], v4 offset:1824\n"
        "v_mfma_f32_16x16x32_bf16 v[74:77], v[142:145], v[46:49], v[74:77]\n"
        "ds_read_b64_tr_b16 v[60:61], v4 offset:1792\n"
        "v_mfma_f32_16x16x32_bf16 v[62:65], v[122:125], v[50:53], v[62:65]\n"
        "v_mfma_f32_16x16x32_bf16 v[66:69], v[126:129], v[50:53], v[66:69]\n"
        "v_mfma_f32_16x16x32_bf16 v[70:73], v[130:133], v[50:53], v[70:73]\n"
        "s_waitcnt vmcnt(2)\n"
        "ds_read_b64_tr_b16 v[46:47], v4 offset:2080\n"
        "v_mfma_f32_16x16x32_bf16 v[74:77], v[134:137], v[50:53], v[74:77]\n"
        "ds_read_b64_tr_b16 v[48:49], v4 offset:2048\n"
        "s_waitcnt lgkmcnt(4)\n"
        "v_mfma_f32_16x16x32_bf16 v[62:65], v[114:117], v[54:57], v[62:65]\n"
        "v_mfma_f32_16x16x32_bf16 v[66:69], v[118:121], v[54:57], v[66:69]\n"
        "v_mfma_f32_16x16x32_bf16 v[70:73], v[122:125], v[54:57], v[70:73]\n"
        "ds_read_b64_tr_b16 v[50:51], v4 offset:2336\n"
        "v_mfma_f32_16x16x32_bf16 v[74:77], v[126:129], v[54:57], v[74:77]\n"
        "ds_read_b64_tr_b16 v[52:53], v4 offset:2304\n"
        "ds_read2st64_b32 v[34:35], v6 offset0:0 offset1:16\n"
        "ds_read2st64_b32 v[36:37], v6 offset0:32 offset1:48\n"
        "ds_read_b64 v[248:249], v235 offset:0\n"
        "v_lshlrev_b32 v38, 16, v236\n"
        "s_waitcnt lgkmcnt(7)\n"
        "v_mfma_f32_16x16x32_bf16 v[62:65], v[106:109], v[58:61], v[62:65]\n"
        "ds_read_b64_tr_b16 v[54:55], v4 offset:32\n"
        "v_and_b32 v39, 0xffff0000, v236\n"
        "v_lshlrev_b32 v40, 16, v237\n"
        "v_mfma_f32_16x16x32_bf16 v[66:69], v[110:113], v[58:61], v[66:69]\n"
        "s_setprio 2\n"
        "ds_read_b64_tr_b16 v[56:57], v4 offset:0\n"
        "v_and_b32 v41, 0xffff0000, v237\n"
        "v_mfma_f32_16x16x32_bf16 v[70:73], v[114:117], v[58:61], v[70:73]\n"
        "v_mfma_f32_16x16x32_bf16 v[74:77], v[118:121], v[58:61], v[74:77]\n"
        "s_waitcnt lgkmcnt(2)\n"
        "v_fma_f32 v42, v38, v248, v249\n"
        "v_fma_f32 v43, v39, v248, v249\n"
        "v_mfma_f32_16x16x32_bf16 v[62:65], v[98:101], v[46:49], v[62:65]\n"
        "ds_read_b64_tr_b16 v[58:59], v4 offset:288\n"
        "v_fma_f32 v44, v40, v248, v249\n"
        "v_fma_f32 v45, v41, v248, v249\n"
        "v_mfma_f32_16x16x32_bf16 v[66:69], v[102:105], v[46:49], v[66:69]\n"
        "ds_read_b64_tr_b16 v[60:61], v4 offset:256\n"
        "s_add_u32 s64, s46, 655360\n"
        "s_addc_u32 s65, s47, 0\n"
        "v_fmac_f32 v38, v42, v34\n"
        "v_mfma_f32_16x16x32_bf16 v[70:73], v[106:109], v[46:49], v[70:73]\n"
        "v_fmac_f32 v39, v43, v35\n"
        "v_fmac_f32 v40, v44, v36\n"
        "v_mfma_f32_16x16x32_bf16 v[74:77], v[110:113], v[46:49], v[74:77]\n"
        "s_setprio 1\n"
        "v_fmac_f32 v41, v45, v37\n"
        "v_cvt_pk_bf16_f32 v34, v38, v39\n"
        "v_mfma_f32_16x16x32_bf16 v[70:73], v[98:101], v[50:53], v[70:73]\n"
        "ds_read_b64_tr_b16 v[46:47], v4 offset:544\n"
        "v_cvt_pk_bf16_f32 v35, v40, v41\n"
        "global_store_dwordx2 v7, v[34:35], s[64:65]\n"
        "v_mfma_f32_16x16x32_bf16 v[74:77], v[102:105], v[50:53], v[74:77]\n"
        "ds_read_b64_tr_b16 v[48:49], v4 offset:512\n"
        "ds_read2st64_b32 v[34:35], v6 offset0:4 offset1:20\n"
        "ds_read2st64_b32 v[36:37], v6 offset0:36 offset1:52\n"
        "ds_read_b64 v[248:249], v235 offset:256\n"
        "v_lshlrev_b32 v38, 16, v238\n"
        "s_waitcnt lgkmcnt(7)\n"
        "v_mfma_f32_16x16x32_bf16 v[78:81], v[178:181], v[54:57], v[12:15]\n"
        "v_and_b32 v39, 0xffff0000, v238\n"
        "v_lshlrev_b32 v40, 16, v239\n"
        "global_load_dwordx2 v[226:227], v13, s[78:79] sc1\n"
        "v_mfma_f32_16x16x32_bf16 v[82:85], v[182:185], v[54:57], v[12:15]\n"
        "v_and_b32 v41, 0xffff0000, v239\n"
        "v_mfma_f32_16x16x32_bf16 v[86:89], v[186:189], v[54:57], v[12:15]\n"
        "ds_read_b64_tr_b16 v[50:51], v4 offset:800\n"
        "v_mfma_f32_16x16x32_bf16 v[90:93], v[190:193], v[54:57], v[12:15]\n"
        "ds_read_b64_tr_b16 v[52:53], v4 offset:768\n"
        "s_waitcnt lgkmcnt(2)\n"
        "v_fma_f32 v42, v38, v248, v249\n"
        "v_fma_f32 v43, v39, v248, v249\n"
        "v_mfma_f32_16x16x32_bf16 v[78:81], v[170:173], v[58:61], v[78:81]\n"
        "v_fma_f32 v44, v40, v248, v249\n"
        "v_fma_f32 v45, v41, v248, v249\n"
        "v_mfma_f32_16x16x32_bf16 v[82:85], v[174:177], v[58:61], v[82:85]\n"
        "s_add_u32 s64, s46, 983040\n"
        "s_addc_u32 s65, s47, 0\n"
        "v_fmac_f32 v38, v42, v34\n"
        "v_mfma_f32_16x16x32_bf16 v[86:89], v[178:181], v[58:61], v[86:89]\n"
        "ds_read_b64_tr_b16 v[54:55], v4 offset:1056\n"
        "v_fmac_f32 v39, v43, v35\n"
        "v_fmac_f32 v40, v44, v36\n"
        "v_mfma_f32_16x16x32_bf16 v[90:93], v[182:185], v[58:61], v[90:93]\n"
        "ds_read_b64_tr_b16 v[56:57], v4 offset:1024\n"
        "v_fmac_f32 v41, v45, v37\n"
        "v_cvt_pk_bf16_f32 v34, v38, v39\n"
        "v_mfma_f32_16x16x32_bf16 v[78:81], v[162:165], v[46:49], v[78:81]\n"
        "v_cvt_pk_bf16_f32 v35, v40, v41\n"
        "global_store_dwordx2 v7, v[34:35], s[64:65]\n"
        "v_mfma_f32_16x16x32_bf16 v[82:85], v[166:169], v[46:49], v[82:85]\n"
        "v_mov_b32 v19, v77\n"
        "v_add_f32_dpp v22, v16, v64 wave_ror:1 row_mask:0xf bank_mask:0xf\n"
        "v_mfma_f32_16x16x32_bf16 v[86:89], v[170:173], v[46:49], v[86:89]\n"
        "ds_read_b64_tr_b16 v[58:59], v4 offset:1312\n"
        "v_add_f32_dpp v23, v65, v68 wave_ror:1 row_mask:0xf bank_mask:0xf\n"
        "v_add_f32_dpp v24, v69, v72 wave_ror:1 row_mask:0xf bank_mask:0xf\n"
        "v_mfma_f32_16x16x32_bf16 v[90:93], v[174:177], v[46:49], v[90:93]\n"
        "ds_read_b64_tr_b16 v[60:61], v4 offset:1280\n"
        "v_add_f32_dpp v20, v73, v76 wave_ror:1 row_mask:0xf bank_mask:0xf\n"
        "v_add_f32_dpp v22, v65, v64 wave_shr:1 row_mask:0xf bank_mask:0xf\n"
        "s_waitcnt lgkmcnt(4)\n"
        "v_mfma_f32_16x16x32_bf16 v[78:81], v[154:157], v[50:53], v[78:81]\n"
        "s_setprio 0\n"
        "v_add_f32_dpp v23, v69, v68 wave_shr:1 row_mask:0xf bank_mask:0xf\n"
        "v_add_f32_dpp v24, v73, v72 wave_shr:1 row_mask:0xf bank_mask:0xf\n"
        "v_mfma_f32_16x16x32_bf16 v[82:85], v[158:161], v[50:53], v[82:85]\n"
        "v_add_f32_dpp v20, v77, v76 wave_shr:1 row_mask:0xf bank_mask:0xf\n"
        "v_add_f32_dpp v64, v17, v63 wave_ror:1 row_mask:0xf bank_mask:0xf\n"
        "v_mfma_f32_16x16x32_bf16 v[86:89], v[162:165], v[50:53], v[86:89]\n"
        "ds_read_b64_tr_b16 v[46:47], v4 offset:1568\n"
        "v_add_f32_dpp v68, v22, v67 wave_ror:1 row_mask:0xf bank_mask:0xf\n"
        "v_add_f32_dpp v72, v23, v71 wave_ror:1 row_mask:0xf bank_mask:0xf\n"
        "v_mfma_f32_16x16x32_bf16 v[90:93], v[166:169], v[50:53], v[90:93]\n"
        "ds_read_b64_tr_b16 v[48:49], v4 offset:1536\n"
        "v_add_f32_dpp v21, v24, v75 wave_ror:1 row_mask:0xf bank_mask:0xf\n"
        "v_add_f32_dpp v64, v22, v63 wave_shr:1 row_mask:0xf bank_mask:0xf\n"
        "s_waitcnt lgkmcnt(4)\n"
        "v_mfma_f32_16x16x32_bf16 v[78:81], v[146:149], v[54:57], v[78:81]\n"
        "v_add_f32_dpp v68, v23, v67 wave_shr:1 row_mask:0xf bank_mask:0xf\n"
        "v_add_f32_dpp v72, v24, v71 wave_shr:1 row_mask:0xf bank_mask:0xf\n"
        "v_mfma_f32_16x16x32_bf16 v[82:85], v[150:153], v[54:57], v[82:85]\n"
        "v_add_f32_dpp v21, v20, v75 wave_shr:1 row_mask:0xf bank_mask:0xf\n"
        "v_add_f32_dpp v63, v18, v62 wave_ror:1 row_mask:0xf bank_mask:0xf\n"
        "v_mfma_f32_16x16x32_bf16 v[86:89], v[154:157], v[54:57], v[86:89]\n"
        "ds_read_b64_tr_b16 v[50:51], v4 offset:1824\n"
        "v_add_f32_dpp v67, v64, v66 wave_ror:1 row_mask:0xf bank_mask:0xf\n"
        "v_add_f32_dpp v71, v68, v70 wave_ror:1 row_mask:0xf bank_mask:0xf\n"
        "v_mfma_f32_16x16x32_bf16 v[90:93], v[158:161], v[54:57], v[90:93]\n"
        "ds_read_b64_tr_b16 v[52:53], v4 offset:1792\n"
        "v_add_f32_dpp v75, v72, v74 wave_ror:1 row_mask:0xf bank_mask:0xf\n"
        "v_add_f32_dpp v63, v64, v62 wave_shr:1 row_mask:0xf bank_mask:0xf\n"
        "s_waitcnt lgkmcnt(4)\n"
        "v_mfma_f32_16x16x32_bf16 v[78:81], v[138:141], v[58:61], v[78:81]\n"
        "v_add_f32_dpp v67, v68, v66 wave_shr:1 row_mask:0xf bank_mask:0xf\n"
        "ds_write2st64_b32 v5, v63, v67 offset0:8 offset1:9\n"
        "v_add_f32_dpp v71, v72, v70 wave_shr:1 row_mask:0xf bank_mask:0xf\n"
        "v_mfma_f32_16x16x32_bf16 v[82:85], v[142:145], v[58:61], v[82:85]\n"
        "v_add_f32_dpp v75, v21, v74 wave_shr:1 row_mask:0xf bank_mask:0xf\n"
        "ds_write2st64_b32 v5, v71, v75 offset0:10 offset1:11\n"
        "v_mfma_f32_16x16x32_bf16 v[86:89], v[146:149], v[58:61], v[86:89]\n"
        "ds_read_b64_tr_b16 v[54:55], v4 offset:2080\n"
        "v_mfma_f32_16x16x32_bf16 v[90:93], v[150:153], v[58:61], v[90:93]\n"
        "ds_read_b64_tr_b16 v[56:57], v4 offset:2048\n"
        "s_waitcnt lgkmcnt(6)\n"
        "v_mfma_f32_16x16x32_bf16 v[78:81], v[130:133], v[46:49], v[78:81]\n"
        "v_mfma_f32_16x16x32_bf16 v[82:85], v[134:137], v[46:49], v[82:85]\n"
        "s_waitcnt lgkmcnt(2)\n"
        "s_barrier\n"
        "s_setprio 3\n"
        "ds_read2st64_b32 v[34:35], v6 offset0:8 offset1:24\n"
        "ds_read2st64_b32 v[36:37], v6 offset0:40 offset1:56\n"
        "v_mfma_f32_16x16x32_bf16 v[86:89], v[138:141], v[46:49], v[86:89]\n"
        "ds_read_b64_tr_b16 v[58:59], v4 offset:2336\n"
        "ds_read_b64 v[248:249], v235 offset:512\n"
        "s_waitcnt vmcnt(4)\n"
        "v_lshlrev_b32 v38, 16, v240\n"
        "v_and_b32 v39, 0xffff0000, v240\n"
        "v_mfma_f32_16x16x32_bf16 v[90:93], v[142:145], v[46:49], v[90:93]\n"
        "ds_read_b64_tr_b16 v[60:61], v4 offset:2304\n"
        "v_lshlrev_b32 v40, 16, v241\n"
        "v_and_b32 v41, 0xffff0000, v241\n"
        "v_mfma_f32_16x16x32_bf16 v[78:81], v[122:125], v[50:53], v[78:81]\n"
        "s_waitcnt vmcnt(1)\n"
        "v_readfirstlane_b32 s52, v226\n"
        "v_readfirstlane_b32 s55, v227\n"
        "v_mfma_f32_16x16x32_bf16 v[82:85], v[126:129], v[50:53], v[82:85]\n"
        "s_waitcnt lgkmcnt(1)\n"
        "v_fma_f32 v42, v38, v248, v249\n"
        "v_fma_f32 v43, v39, v248, v249\n"
        "v_mfma_f32_16x16x32_bf16 v[86:89], v[130:133], v[50:53], v[86:89]\n"
        "ds_read_b64_tr_b16 v[46:47], v4 offset:2592\n"
        "v_fma_f32 v44, v40, v248, v249\n"
        "v_fma_f32 v45, v41, v248, v249\n"
        "v_mfma_f32_16x16x32_bf16 v[90:93], v[134:137], v[50:53], v[90:93]\n"
        "ds_read_b64_tr_b16 v[48:49], v4 offset:2560\n"
        "s_add_u32 s64, s46, 1310720\n"
        "s_addc_u32 s65, s47, 0\n"
        "v_fmac_f32 v38, v42, v34\n"
        "v_mfma_f32_16x16x32_bf16 v[78:81], v[114:117], v[54:57], v[78:81]\n"
        "s_bfe_u32 s60, s52, 0x60005\n"
        "s_bitcmp1_b64 s[76:77], s60\n"
        "s_cselect_b32 s59, 1, 0\n"
        "v_fmac_f32 v39, v43, v35\n"
        "v_fmac_f32 v40, v44, v36\n"
        "v_mfma_f32_16x16x32_bf16 v[82:85], v[118:121], v[54:57], v[82:85]\n"
        "s_bfe_u32 s60, s52, 0x60015\n"
        "s_bitcmp1_b64 s[76:77], s60\n"
        "s_cselect_b32 s60, 2, 0\n"
        "s_or_b32 s59, s59, s60\n"
        "v_fmac_f32 v41, v45, v37\n"
        "v_mfma_f32_16x16x32_bf16 v[86:89], v[122:125], v[54:57], v[86:89]\n"
        "ds_read_b64_tr_b16 v[50:51], v4 offset:2848\n"
        "s_cmp_eq_u32 s55, s31\n"
        "s_cselect_b32 s59, s59, 4\n"
        "v_cvt_pk_bf16_f32 v34, v38, v39\n"
        "v_cvt_pk_bf16_f32 v35, v40, v41\n"
        "v_mfma_f32_16x16x32_bf16 v[90:93], v[126:129], v[54:57], v[90:93]\n"
        "ds_read_b64_tr_b16 v[52:53], v4 offset:2816\n"
        "global_store_dwordx2 v7, v[34:35], s[64:65]\n"
        "s_waitcnt lgkmcnt(4)\n"
        "v_mfma_f32_16x16x32_bf16 v[78:81], v[106:109], v[58:61], v[78:81]\n"
        "v_mfma_f32_16x16x32_bf16 v[82:85], v[110:113], v[58:61], v[82:85]\n"
        "ds_read_b64 v[248:249], v235 offset:768\n"
        "v_lshlrev_b32 v38, 16, v242\n"
        "v_and_b32 v39, 0xffff0000, v242\n"
        "v_mfma_f32_16x16x32_bf16 v[86:89], v[114:117], v[58:61], v[86:89]\n"
        "v_lshlrev_b32 v40, 16, v243\n"
        "v_and_b32 v41, 0xffff0000, v243\n"
        "v_mfma_f32_16x16x32_bf16 v[90:93], v[118:121], v[58:61], v[90:93]\n"
        "s_waitcnt lgkmcnt(3)\n"
        "v_mfma_f32_16x16x32_bf16 v[78:81], v[98:101], v[46:49], v[78:81]\n"
        "s_waitcnt lgkmcnt(0)\n"
        "v_fma_f32 v42, v38, v248, v249\n"
        "v_fma_f32 v43, v39, v248, v249\n"
        "s_setprio 2\n"
        "v_mfma_f32_16x16x32_bf16 v[82:85], v[102:105], v[46:49], v[82:85]\n"
        "v_fma_f32 v44, v40, v248, v249\n"
        "v_fma_f32 v45, v41, v248, v249\n"
        "v_mfma_f32_16x16x32_bf16 v[86:89], v[106:109], v[46:49], v[86:89]\n"
        "s_add_u32 s64, s46, 1638400\n"
        "s_addc_u32 s65, s47, 0\n"
        "s_setprio 1\n"
        "v_mfma_f32_16x16x32_bf16 v[90:93], v[110:113], v[46:49], v[90:93]\n"
        "v_mfma_f32_16x16x32_bf16 v[86:89], v[98:101], v[50:53], v[86:89]\n"
        "s_setprio 0\n"
        "v_mfma_f32_16x16x32_bf16 v[90:93], v[102:105], v[50:53], v[90:93]\n"
        "v_add_f32_dpp v22, v19, v80 wave_ror:1 row_mask:0xf bank_mask:0xf\n"
        "v_add_f32_dpp v23, v81, v84 wave_ror:1 row_mask:0xf bank_mask:0xf\n"
        "s_nop 3\n"
        "v_add_f32_dpp v24, v85, v88 wave_ror:1 row_mask:0xf bank_mask:0xf\n"
        "s_nop 0\n"
        "v_add_f32_dpp v17, v89, v92 wave_ror:1 row_mask:0xf bank_mask:0xf\n"
        "v_add_f32_dpp v22, v81, v80 wave_shr:1 row_mask:0xf bank_mask:0xf\n"
        "v_add_f32_dpp v23, v85, v84 wave_shr:1 row_mask:0xf bank_mask:0xf\n"
        "v_add_f32_dpp v24, v89, v88 wave_shr:1 row_mask:0xf bank_mask:0xf\n"
        "v_add_f32_dpp v17, v93, v92 wave_shr:1 row_mask:0xf bank_mask:0xf\n"
        "v_add_f32_dpp v80, v20, v79 wave_ror:1 row_mask:0xf bank_mask:0xf\n"
        "v_add_f32_dpp v84, v22, v83 wave_ror:1 row_mask:0xf bank_mask:0xf\n"
        "v_add_f32_dpp v88, v23, v87 wave_ror:1 row_mask:0xf bank_mask:0xf\n"
        "v_add_f32_dpp v18, v24, v91 wave_ror:1 row_mask:0xf bank_mask:0xf\n"
        "v_add_f32_dpp v80, v22, v79 wave_shr:1 row_mask:0xf bank_mask:0xf\n"
        "v_add_f32_dpp v84, v23, v83 wave_shr:1 row_mask:0xf bank_mask:0xf\n"
        "v_add_f32_dpp v88, v24, v87 wave_shr:1 row_mask:0xf bank_mask:0xf\n"
        "v_add_f32_dpp v18, v17, v91 wave_shr:1 row_mask:0xf bank_mask:0xf\n"
        "v_add_f32_dpp v79, v21, v78 wave_ror:1 row_mask:0xf bank_mask:0xf\n"
        "v_add_f32_dpp v83, v80, v82 wave_ror:1 row_mask:0xf bank_mask:0xf\n"
        "v_add_f32_dpp v87, v84, v86 wave_ror:1 row_mask:0xf bank_mask:0xf\n"
        "v_add_f32_dpp v91, v88, v90 wave_ror:1 row_mask:0xf bank_mask:0xf\n"
        "v_add_f32_dpp v79, v80, v78 wave_shr:1 row_mask:0xf bank_mask:0xf\n"
        "v_add_f32_dpp v83, v84, v82 wave_shr:1 row_mask:0xf bank_mask:0xf\n"
        "ds_write2st64_b32 v5, v79, v83 offset0:12 offset1:13\n"
        "v_add_f32_dpp v87, v88, v86 wave_shr:1 row_mask:0xf bank_mask:0xf\n"
        "v_add_f32_dpp v91, v18, v90 wave_shr:1 row_mask:0xf bank_mask:0xf\n"
        "ds_write2st64_b32 v5, v87, v91 offset0:14 offset1:15\n"
        "s_and_b32 s55, s59, 3\n"
        "s_cbranch_scc0 .LconvE_ne_h3\n"
        "s_bitcmp1_b32 s59, 0\n"
        "s_cselect_b32 s60, 0, 16\n"
        "s_cselect_b32 s61, 0, 5120\n"
        "s_add_u32 s66, s14, s61\n"
        "s_addc_u32 s67, s15, 0\n"
        "s_lshr_b32 s60, s52, s60\n"
        "s_and_b32 s60, s60, 0x7ff\n"
        "s_lshl_b32 s62, s60, 3\n"
        "s_add_u32 s68, s48, s62\n"
        "s_addc_u32 s69, s49, 0\n"
        "s_mul_i32 s63, s60, 1280\n"
        "v_lshl_add_u32 v170, v232, 2, s63\n"
        "s_lshr_b32 s61, s60, 8\n"
        "s_lshl_b32 s61, s61, 2\n"
        "s_lshr_b32 s61, s74, s61\n"
        "s_and_b32 s61, s61, 15\n"
        "s_lshl_b32 s61, s61, 8\n"
        "s_and_b32 s62, s60, 255\n"
        "s_add_u32 s61, s61, s62\n"
        "s_lshl_b32 s61, s61, 2\n"
        "v_add_u32 v195, s61, v251\n"
        "global_load_dwordx4 v[178:181], v228, s[66:67] sc1\n"
        "global_load_dwordx4 v[182:185], v229, s[66:67] sc1\n"
        "global_load_dwordx4 v[186:189], v230, s[66:67] sc1\n"
        "global_load_dwordx4 v[190:193], v231, s[66:67] sc1\n"
        "global_load_dwordx2 v[174:175], v13, s[68:69] sc1\n"
        "global_load_dword v171, v170, s[44:45]\n"
        ".LconvE_ne_h3:\n"
        "s_waitcnt lgkmcnt(0)\n"
        "s_barrier\n"
        "ds_read2st64_b32 v[34:35], v6 offset0:12 offset1:28\n"
        "ds_read2st64_b32 v[36:37], v6 offset0:44 offset1:60\n"
        "s_waitcnt lgkmcnt(1)\n"
        "v_fmac_f32 v38, v42, v34\n"
        "v_fmac_f32 v39, v43, v35\n"
        "s_waitcnt lgkmcnt(0)\n"
        "v_fmac_f32 v40, v44, v36\n"
        "v_fmac_f32 v41, v45, v37\n"
        "v_cvt_pk_bf16_f32 v34, v38, v39\n"
        "v_cvt_pk_bf16_f32 v35, v40, v41\n"
        "global_store_dwordx2 v7, v[34:35], s[64:65]\n"
        "s_waitcnt vmcnt(1)\n"
        "s_branch .Lrouted_h3\n"
        ".LconvL_h3:\n"
        "v_and_b32 v232, 3, v1\n"
        "v_lshrrev_b32 v24, 1, v232\n"
        "v_lshlrev_b32 v24, 6, v24\n"
        "v_lshl_add_u32 v251, v232, 13, v24\n"
        "v_add_u32 v251, 110592, v251\n"
        "v_lshrrev_b32 v24, 2, v1\n"
        "v_cmp_gt_u32 s[80:81], 6, v24\n"
        "s_lshl_b32 s54, s87, 6\n"
        "s_add_u32 s54, s54, 65536\n"
        "v_lshl_add_u32 v25, v232, 4, s54\n"
        "s_movk_i32 s54, 10240\n"
        "v_mad_u32_u24 v228, v24, s54, v25\n"
        "v_add_u32 v229, 163840, v228\n"
        "v_add_u32 v230, 327680, v228\n"
        "v_min_u32 v24, 5, v24\n"
        "v_mad_u32_u24 v231, v24, s54, v25\n"
        "v_add_u32 v231, 491520, v231\n"
        "s_lshl_b32 s53, s29, 3\n"
        "s_mov_b32 s54, 0x6040200\n"
        "s_mov_b32 s55, 0x17120d08\n"
        "s_lshr_b64 s[54:55], s[54:55], s53\n"
        "s_and_b32 s89, s54, 0xff\n"
        "s_lshl_b32 s52, s89, 8\n"
        "s_add_u32 s52, s52, 40960\n"
        "s_add_u32 s78, s14, s52\n"
        "s_addc_u32 s79, s15, 0\n"
        "v_lshrrev_b32 v24, 4, v1\n"
        "v_lshlrev_b32 v24, 2, v24\n"
        "s_mov_b32 s52, 0x7610\n"
        "v_bfe_u32 v24, s52, v24, 4\n"
        "v_lshl_add_u32 v8, v24, 11, v8\n"
        "s_mov_b32 s76, 0x101\n"
        "s_mov_b32 s77, 0x1010000\n"
        "s_lshl_b64 s[76:77], s[76:77], s28\n"
        "s_mov_b32 s74, 0x32000010\n"
        "s_cmp_eq_u32 s28, 0\n"
        "s_cbranch_scc1 .LconvL_p0_h3\n"
        "s_waitcnt vmcnt(5)\n"
        "ds_read_b64_tr_b16 v[106:107], v3 offset:256\n"
        "ds_read_b64_tr_b16 v[108:109], v3 offset:288\n"
        "ds_read_b64_tr_b16 v[110:111], v3 offset:384\n"
        "ds_read_b64_tr_b16 v[112:113], v3 offset:416\n"
        "ds_read_b64_tr_b16 v[114:115], v3 offset:512\n"
        "ds_read_b64_tr_b16 v[116:117], v3 offset:544\n"
        "ds_read_b64_tr_b16 v[118:119], v3 offset:640\n"
        "ds_read_b64_tr_b16 v[120:121], v3 offset:672\n"
        "ds_read_b64_tr_b16 v[122:123], v3 offset:768\n"
        "ds_read_b64_tr_b16 v[124:125], v3 offset:800\n"
        "ds_read_b64_tr_b16 v[126:127], v3 offset:896\n"
        "ds_read_b64_tr_b16 v[128:129], v3 offset:928\n"
        "s_waitcnt vmcnt(4)\n"
        "ds_read_b64_tr_b16 v[202:203], v3 offset:3328\n"
        "ds_read_b64_tr_b16 v[204:205], v3 offset:3360\n"
        "ds_read_b64_tr_b16 v[206:207], v3 offset:3456\n"
        "ds_read_b64_tr_b16 v[208:209], v3 offset:3488\n"
        "ds_read_b64_tr_b16 v[210:211], v3 offset:3584\n"
        "ds_read_b64_tr_b16 v[212:213], v3 offset:3616\n"
        "ds_read_b64_tr_b16 v[214:215], v3 offset:3712\n"
        "ds_read_b64_tr_b16 v[216:217], v3 offset:3744\n"
        "ds_read_b64_tr_b16 v[218:219], v3 offset:3840\n"
        "ds_read_b64_tr_b16 v[220:221], v3 offset:3872\n"
        "ds_read_b64_tr_b16 v[222:223], v3 offset:3968\n"
        "ds_read_b64_tr_b16 v[224:225], v3 offset:4000\n"
        "s_branch .LconvL_np_h3\n"
        ".LconvL_p0_h3:\n"
        "s_mov_b64 exec, 65535\n"
        "s_mov_b32 s86, 32768\n"
        "global_atomic_or v236, v9, v13, s[68:69] sc0 sc1\n"
        "s_sleep 5\n"
        "global_atomic_or v237, v9, v13, s[68:69] sc0 sc1\n"
        "s_sleep 5\n"
        "global_atomic_or v238, v9, v13, s[68:69] sc0 sc1\n"
        ".LconvL_pl_h3:\n"
        "s_waitcnt vmcnt(2)\n"
        "v_cmp_ne_u32 vcc, s31, v236\n"
        "s_nop 0\n"
        "s_cbranch_vccz .LconvL_px_h3\n"
        "global_atomic_or v236, v9, v13, s[68:69] sc0 sc1\n"
        "s_waitcnt vmcnt(2)\n"
        "v_cmp_ne_u32 vcc, s31, v237\n"
        "s_nop 0\n"
        "s_cbranch_vccz .LconvL_px_h3\n"
        "global_atomic_or v237, v9, v13, s[68:69] sc0 sc1\n"
        "s_waitcnt vmcnt(2)\n"
        "v_cmp_ne_u32 vcc, s31, v238\n"
        "s_nop 0\n"
        "s_cbranch_vccz .LconvL_px_h3\n"
        "global_atomic_or v238, v9, v13, s[68:69] sc0 sc1\n"
        "s_sub_u32 s86, s86, 1\n"
        "s_cmp_lg_u32 s86, 0\n"
        "s_cbranch_scc1 .LconvL_pl_h3\n"
        ".LconvL_px_h3:\n"
        "s_mov_b64 exec, -1\n"
        ".LconvL_np_h3:\n"
        "s_barrier\n"
        "s_mov_b32 m0, s51\n"
        "s_nop 0\n"
        "global_load_lds_dwordx4 v2, s[66:67] offset:0 sc1\n"
        "s_cmp_lg_u32 s28, 0\n"
        "s_cbranch_scc1 .LconvL_n2_h3\n"
        "ds_read_b64_tr_b16 v[106:107], v3 offset:256\n"
        "ds_read_b64_tr_b16 v[108:109], v3 offset:288\n"
        "ds_read_b64_tr_b16 v[110:111], v3 offset:384\n"
        "ds_read_b64_tr_b16 v[112:113], v3 offset:416\n"
        "ds_read_b64_tr_b16 v[114:115], v3 offset:512\n"
        "ds_read_b64_tr_b16 v[116:117], v3 offset:544\n"
        "ds_read_b64_tr_b16 v[118:119], v3 offset:640\n"
        "ds_read_b64_tr_b16 v[120:121], v3 offset:672\n"
        "ds_read_b64_tr_b16 v[122:123], v3 offset:768\n"
        "ds_read_b64_tr_b16 v[124:125], v3 offset:800\n"
        "ds_read_b64_tr_b16 v[126:127], v3 offset:896\n"
        "ds_read_b64_tr_b16 v[128:129], v3 offset:928\n"
        "ds_read_b64_tr_b16 v[202:203], v3 offset:3328\n"
        "ds_read_b64_tr_b16 v[204:205], v3 offset:3360\n"
        "ds_read_b64_tr_b16 v[206:207], v3 offset:3456\n"
        "ds_read_b64_tr_b16 v[208:209], v3 offset:3488\n"
        "ds_read_b64_tr_b16 v[210:211], v3 offset:3584\n"
        "ds_read_b64_tr_b16 v[212:213], v3 offset:3616\n"
        "ds_read_b64_tr_b16 v[214:215], v3 offset:3712\n"
        "ds_read_b64_tr_b16 v[216:217], v3 offset:3744\n"
        "ds_read_b64_tr_b16 v[218:219], v3 offset:3840\n"
        "ds_read_b64_tr_b16 v[220:221], v3 offset:3872\n"
        "ds_read_b64_tr_b16 v[222:223], v3 offset:3968\n"
        "ds_read_b64_tr_b16 v[224:225], v3 offset:4000\n"
        ".LconvL_n2_h3:\n"
        "s_waitcnt vmcnt(2)\n"
        "s_mov_b64 exec, 1\n"
        "global_atomic_swap v9, v226, s[72:73] offset:128 sc1\n"
        "s_mov_b64 exec, -1\n"
        "v_mov_b32 v38, s50\n"
        "s_mov_b64 exec, 1\n"
        "ds_read_u16 v39, v38\n"
        "v_lshlrev_b32 v10, 16, v10\n"
        "s_waitcnt lgkmcnt(0)\n"
        "v_lshlrev_b32 v39, 16, v39\n"
        "v_add_f32 v10, v10, v39\n"
        "v_cvt_pk_bf16_f32 v10, v10, v10\n"
        "ds_write_b16 v38, v10\n"
        "s_mov_b64 exec, -1\n"
        "s_mov_b32 m0, s51\n"
        "s_nop 0\n"
        "global_load_lds_dwordx4 v2, s[66:67] offset:1024 sc1\n"
        "ds_read_b64_tr_b16 v[98:99], v3 offset:0\n"
        "ds_read_b64_tr_b16 v[100:101], v3 offset:32\n"
        "ds_read_b64_tr_b16 v[102:103], v3 offset:128\n"
        "ds_read_b64_tr_b16 v[104:105], v3 offset:160\n"
        "s_waitcnt vmcnt(3)\n"
        "ds_read_b64_tr_b16 v[194:195], v3 offset:3072\n"
        "ds_read_b64_tr_b16 v[196:197], v3 offset:3104\n"
        "ds_read_b64_tr_b16 v[198:199], v3 offset:3200\n"
        "ds_read_b64_tr_b16 v[200:201], v3 offset:3232\n"
        "ds_read_b64_tr_b16 v[186:187], v3 offset:2816\n"
        "ds_read_b64_tr_b16 v[188:189], v3 offset:2848\n"
        "ds_read_b64_tr_b16 v[190:191], v3 offset:2944\n"
        "ds_read_b64_tr_b16 v[192:193], v3 offset:2976\n"
        "ds_read_b64_tr_b16 v[178:179], v3 offset:2560\n"
        "ds_read_b64_tr_b16 v[180:181], v3 offset:2592\n"
        "ds_read_b64_tr_b16 v[182:183], v3 offset:2688\n"
        "ds_read_b64_tr_b16 v[184:185], v3 offset:2720\n"
        "ds_read_b64_tr_b16 v[170:171], v3 offset:2304\n"
        "ds_read_b64_tr_b16 v[172:173], v3 offset:2336\n"
        "ds_read_b64_tr_b16 v[174:175], v3 offset:2432\n"
        "ds_read_b64_tr_b16 v[176:177], v3 offset:2464\n"
        "s_mov_b32 m0, s50\n"
        "s_nop 0\n"
        "global_load_lds_dwordx4 v2, s[40:41] offset:1024\n"
        "s_waitcnt vmcnt(3)\n"
        "ds_read_b64_tr_b16 v[46:47], v4 offset:32\n"
        "ds_read_b64_tr_b16 v[48:49], v4 offset:0\n"
        "ds_read_b64_tr_b16 v[50:51], v4 offset:288\n"
        "ds_read_b64_tr_b16 v[52:53], v4 offset:256\n"
        "ds_read_b64_tr_b16 v[54:55], v4 offset:32\n"
        "ds_read_b64_tr_b16 v[56:57], v4 offset:0\n"
        "s_setprio 3\n"
        "s_waitcnt lgkmcnt(4)\n"
        "v_mfma_f32_16x16x32_bf16 v[62:65], v[98:101], v[46:49], v[12:15]\n"
        "ds_read_b64_tr_b16 v[58:59], v4 offset:288\n"
        "s_add_u32 s42, s44, 0\n"
        "s_addc_u32 s43, s45, 0\n"
        "global_load_dwordx2 v[236:237], v7, s[42:43]\n"
        "v_mfma_f32_16x16x32_bf16 v[66:69], v[102:105], v[46:49], v[12:15]\n"
        "ds_read_b64_tr_b16 v[60:61], v4 offset:256\n"
        "v_mfma_f32_16x16x32_bf16 v[70:73], v[106:109], v[46:49], v[12:15]\n"
        "v_mfma_f32_16x16x32_bf16 v[74:77], v[110:113], v[46:49], v[12:15]\n"
        "s_waitcnt lgkmcnt(4)\n"
        "v_mfma_f32_16x16x32_bf16 v[70:73], v[98:101], v[50:53], v[70:73]\n"
        "ds_read_b64_tr_b16 v[46:47], v4 offset:544\n"
        "v_mfma_f32_16x16x32_bf16 v[74:77], v[102:105], v[50:53], v[74:77]\n"
        "ds_read_b64_tr_b16 v[48:49], v4 offset:512\n"
        "s_waitcnt lgkmcnt(4)\n"
        "v_mfma_f32_16x16x32_bf16 v[78:81], v[114:117], v[54:57], v[12:15]\n"
        "v_mfma_f32_16x16x32_bf16 v[82:85], v[118:121], v[54:57], v[12:15]\n"
        "s_nop 3\n"
        "v_mov_b32 v19, v77\n"
        "v_add_f32_dpp v22, v65, v64 wave_shr:1 row_mask:0xf bank_mask:0xf bound_ctrl:1\n"
        "v_mfma_f32_16x16x32_bf16 v[86:89], v[122:125], v[54:57], v[12:15]\n"
        "ds_read_b64_tr_b16 v[50:51], v4 offset:800\n"
        "v_add_f32_dpp v23, v65, v68 wave_ror:1 row_mask:0xf bank_mask:0xf\n"
        "v_add_f32_dpp v24, v69, v72 wave_ror:1 row_mask:0xf bank_mask:0xf\n"
        "v_mfma_f32_16x16x32_bf16 v[90:93], v[126:129], v[54:57], v[12:15]\n"
        "ds_read_b64_tr_b16 v[52:53], v4 offset:768\n"
        "v_add_f32_dpp v20, v73, v76 wave_ror:1 row_mask:0xf bank_mask:0xf\n"
        "v_add_f32_dpp v23, v69, v68 wave_shr:1 row_mask:0xf bank_mask:0xf\n"
        "s_waitcnt lgkmcnt(4)\n"
        "v_mfma_f32_16x16x32_bf16 v[78:81], v[106:109], v[58:61], v[78:81]\n"
        "ds_read_b64_tr_b16 v[54:55], v4 offset:32\n"
        "v_add_f32_dpp v24, v73, v72 wave_shr:1 row_mask:0xf bank_mask:0xf\n"
        "v_add_f32_dpp v20, v77, v76 wave_shr:1 row_mask:0xf bank_mask:0xf\n"
        "v_mfma_f32_16x16x32_bf16 v[82:85], v[110:113], v[58:61], v[82:85]\n"
        "ds_read_b64_tr_b16 v[56:57], v4 offset:0\n"
        "v_add_f32_dpp v64, v22, v63 wave_shr:1 row_mask:0xf bank_mask:0xf bound_ctrl:1\n"
        "v_add_f32_dpp v68, v22, v67 wave_ror:1 row_mask:0xf bank_mask:0xf\n"
        "v_mfma_f32_16x16x32_bf16 v[86:89], v[114:117], v[58:61], v[86:89]\n"
        "v_add_f32_dpp v72, v23, v71 wave_ror:1 row_mask:0xf bank_mask:0xf\n"
        "v_add_f32_dpp v21, v24, v75 wave_ror:1 row_mask:0xf bank_mask:0xf\n"
        "v_mfma_f32_16x16x32_bf16 v[90:93], v[118:121], v[58:61], v[90:93]\n"
        "v_add_f32_dpp v68, v23, v67 wave_shr:1 row_mask:0xf bank_mask:0xf\n"
        "v_add_f32_dpp v72, v24, v71 wave_shr:1 row_mask:0xf bank_mask:0xf\n"
        "s_waitcnt lgkmcnt(4)\n"
        "v_mfma_f32_16x16x32_bf16 v[78:81], v[98:101], v[46:49], v[78:81]\n"
        "s_setprio 2\n"
        "ds_read_b64_tr_b16 v[58:59], v4 offset:288\n"
        "v_add_f32_dpp v21, v20, v75 wave_shr:1 row_mask:0xf bank_mask:0xf\n"
        "v_add_f32_dpp v63, v64, v62 wave_shr:1 row_mask:0xf bank_mask:0xf bound_ctrl:1\n"
        "v_mfma_f32_16x16x32_bf16 v[82:85], v[102:105], v[46:49], v[82:85]\n"
        "ds_read_b64_tr_b16 v[60:61], v4 offset:256\n"
        "v_add_f32_dpp v67, v64, v66 wave_ror:1 row_mask:0xf bank_mask:0xf\n"
        "v_add_f32_dpp v71, v68, v70 wave_ror:1 row_mask:0xf bank_mask:0xf\n"
        "v_mfma_f32_16x16x32_bf16 v[86:89], v[106:109], v[46:49], v[86:89]\n"
        "v_add_f32_dpp v75, v72, v74 wave_ror:1 row_mask:0xf bank_mask:0xf\n"
        "v_add_f32_dpp v67, v68, v66 wave_shr:1 row_mask:0xf bank_mask:0xf\n"
        "v_mfma_f32_16x16x32_bf16 v[90:93], v[110:113], v[46:49], v[90:93]\n"
        "ds_write2st64_b32 v5, v63, v67 offset0:0 offset1:1\n"
        "v_add_f32_dpp v71, v72, v70 wave_shr:1 row_mask:0xf bank_mask:0xf\n"
        "v_add_f32_dpp v75, v21, v74 wave_shr:1 row_mask:0xf bank_mask:0xf\n"
        "s_waitcnt lgkmcnt(5)\n"
        "v_mfma_f32_16x16x32_bf16 v[86:89], v[98:101], v[50:53], v[86:89]\n"
        "ds_write2st64_b32 v5, v71, v75 offset0:2 offset1:3\n"
        "v_mfma_f32_16x16x32_bf16 v[90:93], v[102:105], v[50:53], v[90:93]\n"
        "ds_read_b64_tr_b16 v[46:47], v4 offset:32\n"
        "s_add_u32 s42, s44, 327680\n"
        "s_addc_u32 s43, s45, 0\n"
        "global_load_dwordx2 v[238:239], v7, s[42:43]\n"
        "s_waitcnt lgkmcnt(5)\n"
        "v_mfma_f32_16x16x32_bf16 v[62:65], v[194:197], v[54:57], v[12:15]\n"
        "ds_read_b64_tr_b16 v[48:49], v4 offset:0\n"
        "s_waitcnt vmcnt(2)\n"
        "ds_read_b64_tr_b16 v[26:27], v233 offset:2944\n"
        "v_mfma_f32_16x16x32_bf16 v[66:69], v[198:201], v[54:57], v[12:15]\n"
        "ds_read_b64_tr_b16 v[28:29], v233 offset:2976\n"
        "v_add_f32_dpp v22, v19, v80 wave_ror:1 row_mask:0xf bank_mask:0xf\n"
        "v_add_f32_dpp v23, v81, v84 wave_ror:1 row_mask:0xf bank_mask:0xf\n"
        "v_mfma_f32_16x16x32_bf16 v[70:73], v[202:205], v[54:57], v[12:15]\n"
        "s_setprio 1\n"
        "ds_read_b64_tr_b16 v[30:31], v234 offset:32\n"
        "v_add_f32_dpp v24, v85, v88 wave_ror:1 row_mask:0xf bank_mask:0xf\n"
        "v_add_f32_dpp v17, v89, v92 wave_ror:1 row_mask:0xf bank_mask:0xf\n"
        "v_mfma_f32_16x16x32_bf16 v[74:77], v[206:209], v[54:57], v[12:15]\n"
        "ds_read_b64_tr_b16 v[50:51], v4 offset:288\n"
        "ds_read_b64_tr_b16 v[32:33], v234 offset:0\n"
        "v_add_f32_dpp v22, v81, v80 wave_shr:1 row_mask:0xf bank_mask:0xf\n"
        "v_add_f32_dpp v23, v85, v84 wave_shr:1 row_mask:0xf bank_mask:0xf\n"
        "s_waitcnt lgkmcnt(9)\n"
        "v_mfma_f32_16x16x32_bf16 v[62:65], v[186:189], v[58:61], v[62:65]\n"
        "ds_read_b64_tr_b16 v[52:53], v4 offset:256\n"
        "s_mov_b64 exec, 0xffff\n"
        "global_atomic_or v227, v9, v13, s[68:69] offset:128 sc0 sc1\n"
        "s_mov_b64 exec, -1\n"
        "v_add_f32_dpp v24, v89, v88 wave_shr:1 row_mask:0xf bank_mask:0xf\n"
        "v_mfma_f32_16x16x32_bf16 v[66:69], v[190:193], v[58:61], v[66:69]\n"
        "v_add_f32_dpp v17, v93, v92 wave_shr:1 row_mask:0xf bank_mask:0xf\n"
        "v_add_f32_dpp v80, v20, v79 wave_ror:1 row_mask:0xf bank_mask:0xf\n"
        "global_load_dwordx4 v[244:247], v8, s[48:49] sc1\n"
        "v_mfma_f32_16x16x32_bf16 v[70:73], v[194:197], v[58:61], v[70:73]\n"
        "v_add_f32_dpp v84, v22, v83 wave_ror:1 row_mask:0xf bank_mask:0xf\n"
        "v_add_f32_dpp v88, v23, v87 wave_ror:1 row_mask:0xf bank_mask:0xf\n"
        "v_mfma_f32_16x16x32_bf16 v[74:77], v[198:201], v[58:61], v[74:77]\n"
        "ds_read_b64_tr_b16 v[54:55], v4 offset:544\n"
        "v_add_f32_dpp v18, v24, v91 wave_ror:1 row_mask:0xf bank_mask:0xf\n"
        "v_add_f32_dpp v80, v22, v79 wave_shr:1 row_mask:0xf bank_mask:0xf\n"
        "s_waitcnt lgkmcnt(2)\n"
        "v_mfma_f32_16x16x32_bf16 v[94:97], v[26:29], v[30:33], 0\n"
        "ds_read_b64_tr_b16 v[56:57], v4 offset:512\n"
        "v_add_f32_dpp v84, v23, v83 wave_shr:1 row_mask:0xf bank_mask:0xf\n"
        "v_add_f32_dpp v88, v24, v87 wave_shr:1 row_mask:0xf bank_mask:0xf\n"
        "v_add_f32_dpp v18, v17, v91 wave_shr:1 row_mask:0xf bank_mask:0xf\n"
        "v_add_f32_dpp v79, v21, v78 wave_ror:1 row_mask:0xf bank_mask:0xf\n"
        "v_add_f32_dpp v83, v80, v82 wave_ror:1 row_mask:0xf bank_mask:0xf\n"
        "v_add_f32_dpp v87, v84, v86 wave_ror:1 row_mask:0xf bank_mask:0xf\n"
        "v_add_f32_dpp v91, v88, v90 wave_ror:1 row_mask:0xf bank_mask:0xf\n"
        "v_add_f32_dpp v79, v80, v78 wave_shr:1 row_mask:0xf bank_mask:0xf\n"
        "v_add_f32_dpp v83, v84, v82 wave_shr:1 row_mask:0xf bank_mask:0xf\n"
        "ds_write2st64_b32 v5, v79, v83 offset0:4 offset1:5\n"
        "v_add_f32_dpp v87, v88, v86 wave_shr:1 row_mask:0xf bank_mask:0xf\n"
        "v_add_f32_dpp v91, v18, v90 wave_shr:1 row_mask:0xf bank_mask:0xf\n"
        "ds_write2st64_b32 v5, v87, v91 offset0:6 offset1:7\n"
        "v_mfma_f32_16x16x32_bf16 v[78:81], v[210:213], v[46:49], v[12:15]\n"
        "s_setprio 0\n"
        "ds_read_b64_tr_b16 v[162:163], v3 offset:2048\n"
        "v_mfma_f32_16x16x32_bf16 v[82:85], v[214:217], v[46:49], v[12:15]\n"
        "ds_read_b64_tr_b16 v[164:165], v3 offset:2080\n"
        "v_mfma_f32_16x16x32_bf16 v[86:89], v[218:221], v[46:49], v[12:15]\n"
        "ds_read_b64_tr_b16 v[166:167], v3 offset:2176\n"
        "v_mfma_f32_16x16x32_bf16 v[90:93], v[222:225], v[46:49], v[12:15]\n"
        "ds_read_b64_tr_b16 v[168:169], v3 offset:2208\n"
        "s_waitcnt lgkmcnt(8)\n"
        "v_mfma_f32_16x16x32_bf16 v[78:81], v[202:205], v[50:53], v[78:81]\n"
        "s_waitcnt lgkmcnt(4)\n"
        "s_barrier\n"
        "s_setprio 3\n"
        "v_mfma_f32_16x16x32_bf16 v[82:85], v[206:209], v[50:53], v[82:85]\n"
        "v_mfma_f32_16x16x32_bf16 v[86:89], v[210:213], v[50:53], v[86:89]\n"
        "ds_read_b64_tr_b16 v[58:59], v4 offset:800\n"
        "v_mfma_f32_16x16x32_bf16 v[90:93], v[214:217], v[50:53], v[90:93]\n"
        "ds_read_b64_tr_b16 v[60:61], v4 offset:768\n"
        "v_mfma_f32_16x16x32_bf16 v[62:65], v[178:181], v[54:57], v[62:65]\n"
        "ds_read_b64_tr_b16 v[154:155], v3 offset:1792\n"
        "v_mfma_f32_16x16x32_bf16 v[66:69], v[182:185], v[54:57], v[66:69]\n"
        "ds_read_b64_tr_b16 v[156:157], v3 offset:1824\n"
        "s_add_u32 s42, s44, 1966080\n"
        "s_addc_u32 s43, s45, 0\n"
        "global_load_dwordx2 v[240:241], v7, s[42:43]\n"
        "v_mfma_f32_16x16x32_bf16 v[70:73], v[186:189], v[54:57], v[70:73]\n"
        "ds_read_b64_tr_b16 v[158:159], v3 offset:1920\n"
        "v_mfma_f32_16x16x32_bf16 v[74:77], v[190:193], v[54:57], v[74:77]\n"
        "ds_read_b64_tr_b16 v[160:161], v3 offset:1952\n"
        "v_mfma_f32_16x16x32_bf16 v[78:81], v[194:197], v[54:57], v[78:81]\n"
        "v_mfma_f32_16x16x32_bf16 v[82:85], v[198:201], v[54:57], v[82:85]\n"
        "v_mfma_f32_16x16x32_bf16 v[86:89], v[202:205], v[54:57], v[86:89]\n"
        "ds_read_b64_tr_b16 v[46:47], v4 offset:1056\n"
        "v_mfma_f32_16x16x32_bf16 v[90:93], v[206:209], v[54:57], v[90:93]\n"
        "ds_read_b64_tr_b16 v[48:49], v4 offset:1024\n"
        "s_waitcnt lgkmcnt(6)\n"
        "v_mfma_f32_16x16x32_bf16 v[62:65], v[170:173], v[58:61], v[62:65]\n"
        "s_waitcnt vmcnt(2)\n"
        "v_cmp_ne_u32 vcc, s31, v227\n"
        "s_nop 0\n"
        "s_cbranch_vccnz .LconvLf1_p_h3\n"
        ".LconvLf1_r_h3:\n"
        "v_mfma_f32_16x16x32_bf16 v[66:69], v[174:177], v[58:61], v[66:69]\n"
        "ds_read_b64_tr_b16 v[146:147], v3 offset:1536\n"
        "s_mov_b32 m0, s51\n"
        "s_nop 0\n"
        "global_load_lds_dwordx4 v2, s[66:67] offset:2048 sc1\n"
        "v_mfma_f32_16x16x32_bf16 v[70:73], v[178:181], v[58:61], v[70:73]\n"
        "ds_read_b64_tr_b16 v[148:149], v3 offset:1568\n"
        "s_mov_b32 m0, s51\n"
        "s_nop 0\n"
        "global_load_lds_dwordx4 v2, s[66:67] offset:3072 sc1\n"
        "v_mfma_f32_16x16x32_bf16 v[74:77], v[182:185], v[58:61], v[74:77]\n"
        "ds_read_b64_tr_b16 v[150:151], v3 offset:1664\n"
        "s_waitcnt vmcnt(3)\n"
        "v_cmp_ne_u32 vcc, s31, v245\n"
        "v_cmp_ne_u32 s[84:85], s31, v247\n"
        "s_nop 0\n"
        "s_or_b64 vcc, vcc, s[84:85]\n"
        "s_cbranch_vccnz .LconvLst_p_h3\n"
        ".LconvLst_r_h3:\n"
        "v_mfma_f32_16x16x32_bf16 v[78:81], v[186:189], v[58:61], v[78:81]\n"
        "ds_read_b64_tr_b16 v[152:153], v3 offset:1696\n"
        "v_lshlrev_b32 v245, 16, v244\n"
        "v_and_b32 v244, 0xffff0000, v244\n"
        "v_lshlrev_b32 v247, 16, v246\n"
        "v_and_b32 v246, 0xffff0000, v246\n"
        "v_mfma_f32_16x16x32_bf16 v[82:85], v[190:193], v[58:61], v[82:85]\n"
        "v_rcp_f32 v244, v244\n"
        "v_rcp_f32 v246, v246\n"
        "v_mul_f32_e64 v245, -v245, v244\n"
        "v_mul_f32_e64 v247, -v247, v246\n"
        "ds_write_b128 v250, v[244:247]\n"
        "v_mfma_f32_16x16x32_bf16 v[86:89], v[194:197], v[58:61], v[86:89]\n"
        "ds_read_b64_tr_b16 v[50:51], v4 offset:1312\n"
        "v_mfma_f32_16x16x32_bf16 v[90:93], v[198:201], v[58:61], v[90:93]\n"
        "ds_read_b64_tr_b16 v[52:53], v4 offset:1280\n"
        "s_waitcnt lgkmcnt(7)\n"
        "v_mfma_f32_16x16x32_bf16 v[62:65], v[162:165], v[46:49], v[62:65]\n"
        "v_mfma_f32_16x16x32_bf16 v[66:69], v[166:169], v[46:49], v[66:69]\n"
        "ds_read_b64_tr_b16 v[138:139], v3 offset:1280\n"
        "v_mfma_f32_16x16x32_bf16 v[70:73], v[170:173], v[46:49], v[70:73]\n"
        "ds_read_b64_tr_b16 v[140:141], v3 offset:1312\n"
        "v_mfma_f32_16x16x32_bf16 v[74:77], v[174:177], v[46:49], v[74:77]\n"
        "ds_read_b64_tr_b16 v[142:143], v3 offset:1408\n"
        "v_mfma_f32_16x16x32_bf16 v[78:81], v[178:181], v[46:49], v[78:81]\n"
        "ds_read_b64_tr_b16 v[144:145], v3 offset:1440\n"
        "v_mfma_f32_16x16x32_bf16 v[82:85], v[182:185], v[46:49], v[82:85]\n"
        "v_mfma_f32_16x16x32_bf16 v[86:89], v[186:189], v[46:49], v[86:89]\n"
        "v_mfma_f32_16x16x32_bf16 v[90:93], v[190:193], v[46:49], v[90:93]\n"
        "ds_read_b64_tr_b16 v[54:55], v4 offset:1568\n"
        "s_add_u32 s42, s44, 2293760\n"
        "s_addc_u32 s43, s45, 0\n"
        "global_load_dwordx2 v[242:243], v7, s[42:43]\n"
        "s_waitcnt lgkmcnt(5)\n"
        "v_mfma_f32_16x16x32_bf16 v[62:65], v[154:157], v[50:53], v[62:65]\n"
        "ds_read_b64_tr_b16 v[56:57], v4 offset:1536\n"
        "ds_read_b64_tr_b16 v[26:27], v233 offset:1920\n"
        "v_mfma_f32_16x16x32_bf16 v[66:69], v[158:161], v[50:53], v[66:69]\n"
        "ds_read_b64_tr_b16 v[28:29], v233 offset:1952\n"
        "v_mfma_f32_16x16x32_bf16 v[70:73], v[162:165], v[50:53], v[70:73]\n"
        "ds_read_b64_tr_b16 v[30:31], v234 offset:1056\n"
        "ds_read2st64_b32 v[34:35], v6 offset0:0 offset1:16\n"
        "ds_read2st64_b32 v[36:37], v6 offset0:32 offset1:48\n"
        "ds_read_b64 v[248:249], v235 offset:0\n"
        "v_lshlrev_b32 v38, 16, v236\n"
        "v_mfma_f32_16x16x32_bf16 v[74:77], v[166:169], v[50:53], v[74:77]\n"
        "ds_read_b64_tr_b16 v[32:33], v234 offset:1024\n"
        "v_and_b32 v39, 0xffff0000, v236\n"
        "v_lshlrev_b32 v40, 16, v237\n"
        "v_mfma_f32_16x16x32_bf16 v[78:81], v[170:173], v[50:53], v[78:81]\n"
        "ds_read_b64_tr_b16 v[130:131], v3 offset:1024\n"
        "v_and_b32 v41, 0xffff0000, v237\n"
        "v_mfma_f32_16x16x32_bf16 v[82:85], v[174:177], v[50:53], v[82:85]\n"
        "ds_read_b64_tr_b16 v[132:133], v3 offset:1056\n"
        "v_mfma_f32_16x16x32_bf16 v[86:89], v[178:181], v[50:53], v[86:89]\n"
        "ds_read_b64_tr_b16 v[134:135], v3 offset:1152\n"
        "s_waitcnt lgkmcnt(4)\n"
        "v_fma_f32 v42, v38, v248, v249\n"
        "v_fma_f32 v43, v39, v248, v249\n"
        "v_mfma_f32_16x16x32_bf16 v[90:93], v[182:185], v[50:53], v[90:93]\n"
        "ds_read_b64_tr_b16 v[58:59], v4 offset:1824\n"
        "ds_read_b64_tr_b16 v[136:137], v3 offset:1184\n"
        "v_fma_f32 v44, v40, v248, v249\n"
        "v_fma_f32 v45, v41, v248, v249\n"
        "s_waitcnt lgkmcnt(5)\n"
        "v_mfma_f32_16x16x32_bf16 v[94:97], v[26:29], v[30:33], v[94:97]\n"
        "s_setprio 2\n"
        "ds_read_b64_tr_b16 v[60:61], v4 offset:1792\n"
        "s_add_u32 s64, s46, 0\n"
        "s_addc_u32 s65, s47, 0\n"
        "v_fmac_f32 v38, v42, v34\n"
        "v_mfma_f32_16x16x32_bf16 v[62:65], v[146:149], v[54:57], v[62:65]\n"
        "v_fmac_f32 v39, v43, v35\n"
        "v_fmac_f32 v40, v44, v36\n"
        "v_mfma_f32_16x16x32_bf16 v[66:69], v[150:153], v[54:57], v[66:69]\n"
        "v_fmac_f32 v41, v45, v37\n"
        "v_cvt_pk_bf16_f32 v34, v38, v39\n"
        "v_mfma_f32_16x16x32_bf16 v[70:73], v[154:157], v[54:57], v[70:73]\n"
        "v_cvt_pk_bf16_f32 v35, v40, v41\n"
        "global_store_dwordx2 v7, v[34:35], s[64:65]\n"
        "v_mfma_f32_16x16x32_bf16 v[74:77], v[158:161], v[54:57], v[74:77]\n"
        "v_mfma_f32_16x16x32_bf16 v[78:81], v[162:165], v[54:57], v[78:81]\n"
        "v_mfma_f32_16x16x32_bf16 v[82:85], v[166:169], v[54:57], v[82:85]\n"
        "v_mfma_f32_16x16x32_bf16 v[86:89], v[170:173], v[54:57], v[86:89]\n"
        "s_waitcnt vmcnt(3)\n"
        "ds_read_b64_tr_b16 v[46:47], v4 offset:2080\n"
        "v_mfma_f32_16x16x32_bf16 v[90:93], v[174:177], v[54:57], v[90:93]\n"
        "ds_read_b64_tr_b16 v[48:49], v4 offset:2048\n"
        "s_waitcnt lgkmcnt(2)\n"
        "v_mfma_f32_16x16x32_bf16 v[62:65], v[138:141], v[58:61], v[62:65]\n"
        "v_mfma_f32_16x16x32_bf16 v[66:69], v[142:145], v[58:61], v[66:69]\n"
        "v_mfma_f32_16x16x32_bf16 v[70:73], v[146:149], v[58:61], v[70:73]\n"
        "ds_read_b64_tr_b16 v[50:51], v4 offset:2336\n"
        "v_mfma_f32_16x16x32_bf16 v[74:77], v[150:153], v[58:61], v[74:77]\n"
        "ds_read_b64_tr_b16 v[52:53], v4 offset:2304\n"
        "v_mfma_f32_16x16x32_bf16 v[78:81], v[154:157], v[58:61], v[78:81]\n"
        "v_mfma_f32_16x16x32_bf16 v[82:85], v[158:161], v[58:61], v[82:85]\n"
        "ds_read2st64_b32 v[34:35], v6 offset0:4 offset1:20\n"
        "ds_read2st64_b32 v[36:37], v6 offset0:36 offset1:52\n"
        "ds_read_b64 v[248:249], v235 offset:256\n"
        "v_lshlrev_b32 v38, 16, v238\n"
        "v_and_b32 v39, 0xffff0000, v238\n"
        "v_mfma_f32_16x16x32_bf16 v[86:89], v[162:165], v[58:61], v[86:89]\n"
        "v_lshlrev_b32 v40, 16, v239\n"
        "v_and_b32 v41, 0xffff0000, v239\n"
        "v_mfma_f32_16x16x32_bf16 v[90:93], v[166:169], v[58:61], v[90:93]\n"
        "ds_read_b64_tr_b16 v[54:55], v4 offset:2592\n"
        "global_load_dwordx2 v[226:227], v13, s[78:79] sc1\n"
        "s_waitcnt lgkmcnt(6)\n"
        "v_mfma_f32_16x16x32_bf16 v[62:65], v[130:133], v[46:49], v[62:65]\n"
        "ds_read_b64_tr_b16 v[56:57], v4 offset:2560\n"
        "ds_read_b64_tr_b16 v[26:27], v233 offset:896\n"
        "s_waitcnt lgkmcnt(3)\n"
        "v_fma_f32 v42, v38, v248, v249\n"
        "v_fma_f32 v43, v39, v248, v249\n"
        "v_mfma_f32_16x16x32_bf16 v[66:69], v[134:137], v[46:49], v[66:69]\n"
        "ds_read_b64_tr_b16 v[28:29], v233 offset:928\n"
        "v_fma_f32 v44, v40, v248, v249\n"
        "v_fma_f32 v45, v41, v248, v249\n"
        "v_mfma_f32_16x16x32_bf16 v[70:73], v[138:141], v[46:49], v[70:73]\n"
        "ds_read_b64_tr_b16 v[30:31], v234 offset:2080\n"
        "s_add_u32 s64, s46, 327680\n"
        "s_addc_u32 s65, s47, 0\n"
        "v_fmac_f32 v38, v42, v34\n"
        "v_mfma_f32_16x16x32_bf16 v[74:77], v[142:145], v[46:49], v[74:77]\n"
        "ds_read_b64_tr_b16 v[58:59], v4 offset:2848\n"
        "ds_read_b64_tr_b16 v[32:33], v234 offset:2048\n"
        "v_fmac_f32 v39, v43, v35\n"
        "v_fmac_f32 v40, v44, v36\n"
        "v_mfma_f32_16x16x32_bf16 v[62:65], v[122:125], v[50:53], v[62:65]\n"
        "ds_read_b64_tr_b16 v[60:61], v4 offset:2816\n"
        "v_fmac_f32 v41, v45, v37\n"
        "v_cvt_pk_bf16_f32 v34, v38, v39\n"
        "v_mfma_f32_16x16x32_bf16 v[66:69], v[126:129], v[50:53], v[66:69]\n"
        "v_cvt_pk_bf16_f32 v35, v40, v41\n"
        "global_store_dwordx2 v7, v[34:35], s[64:65]\n"
        "v_mfma_f32_16x16x32_bf16 v[70:73], v[130:133], v[50:53], v[70:73]\n"
        "v_mfma_f32_16x16x32_bf16 v[74:77], v[134:137], v[50:53], v[74:77]\n"
        "s_waitcnt vmcnt(4)\n"
        "ds_read_b64_tr_b16 v[46:47], v4 offset:3104\n"
        "s_waitcnt lgkmcnt(2)\n"
        "v_mfma_f32_16x16x32_bf16 v[94:97], v[26:29], v[30:33], v[94:97]\n"
        "ds_read_b64_tr_b16 v[48:49], v4 offset:3072\n"
        "v_mfma_f32_16x16x32_bf16 v[62:65], v[114:117], v[54:57], v[62:65]\n"
        "v_mfma_f32_16x16x32_bf16 v[66:69], v[118:121], v[54:57], v[66:69]\n"
        "v_mfma_f32_16x16x32_bf16 v[70:73], v[122:125], v[54:57], v[70:73]\n"
        "ds_read_b64_tr_b16 v[50:51], v4 offset:3360\n"
        "s_nop 2\n"
        "v_add_f32_dpp v96, v97, v96 wave_shr:1 row_mask:0xf bank_mask:0xf bound_ctrl:1\n"
        "v_readlane_b32 s70, v97, 3\n"
        "v_mfma_f32_16x16x32_bf16 v[74:77], v[126:129], v[54:57], v[74:77]\n"
        "ds_read_b64_tr_b16 v[52:53], v4 offset:3328\n"
        "v_readlane_b32 s71, v97, 23\n"
        "v_readlane_b32 s72, v97, 43\n"
        "s_waitcnt lgkmcnt(4)\n"
        "v_mfma_f32_16x16x32_bf16 v[62:65], v[106:109], v[58:61], v[62:65]\n"
        "ds_read_b64_tr_b16 v[54:55], v4 offset:2080\n"
        "v_add_f32_dpp v95, v96, v95 wave_shr:1 row_mask:0xf bank_mask:0xf bound_ctrl:1\n"
        "v_add_f32 v97, s70, v97\n"
        "v_mfma_f32_16x16x32_bf16 v[66:69], v[110:113], v[58:61], v[66:69]\n"
        "ds_read_b64_tr_b16 v[56:57], v4 offset:2048\n"
        "v_add_f32 v97, s71, v97\n"
        "v_add_f32 v97, s72, v97\n"
        "v_mfma_f32_16x16x32_bf16 v[70:73], v[114:117], v[58:61], v[70:73]\n"
        "s_setprio 1\n"
        "v_readlane_b32 s70, v96, 3\n"
        "v_readlane_b32 s71, v96, 23\n"
        "v_mfma_f32_16x16x32_bf16 v[74:77], v[118:121], v[58:61], v[74:77]\n"
        "v_readlane_b32 s72, v96, 43\n"
        "v_add_f32 v96, s70, v96\n"
        "s_waitcnt lgkmcnt(4)\n"
        "v_mfma_f32_16x16x32_bf16 v[62:65], v[98:101], v[46:49], v[62:65]\n"
        "ds_read_b64_tr_b16 v[58:59], v4 offset:2336\n"
        "v_add_f32 v96, s71, v96\n"
        "v_add_f32 v96, s72, v96\n"
        "v_mfma_f32_16x16x32_bf16 v[66:69], v[102:105], v[46:49], v[66:69]\n"
        "ds_read_b64_tr_b16 v[60:61], v4 offset:2304\n"
        "v_readlane_b32 s70, v95, 3\n"
        "v_readlane_b32 s71, v95, 23\n"
        "v_mfma_f32_16x16x32_bf16 v[70:73], v[106:109], v[46:49], v[70:73]\n"
        "v_readlane_b32 s72, v95, 43\n"
        "v_add_f32 v95, s70, v95\n"
        "v_mfma_f32_16x16x32_bf16 v[74:77], v[110:113], v[46:49], v[74:77]\n"
        "v_add_f32 v95, s71, v95\n"
        "v_add_f32 v95, s72, v95\n"
        "s_waitcnt lgkmcnt(4)\n"
        "v_mfma_f32_16x16x32_bf16 v[70:73], v[98:101], v[50:53], v[70:73]\n"
        "ds_read_b64_tr_b16 v[46:47], v4 offset:2592\n"
        "ds_read_b64 v[248:249], v235 offset:512\n"
        "v_lshlrev_b32 v38, 16, v240\n"
        "v_and_b32 v39, 0xffff0000, v240\n"
        "v_mfma_f32_16x16x32_bf16 v[74:77], v[102:105], v[50:53], v[74:77]\n"
        "ds_read_b64_tr_b16 v[48:49], v4 offset:2560\n"
        "v_lshlrev_b32 v40, 16, v241\n"
        "v_and_b32 v41, 0xffff0000, v241\n"
        "s_waitcnt lgkmcnt(5)\n"
        "v_mfma_f32_16x16x32_bf16 v[78:81], v[146:149], v[54:57], v[78:81]\n"
        "v_mfma_f32_16x16x32_bf16 v[82:85], v[150:153], v[54:57], v[82:85]\n"
        "s_waitcnt lgkmcnt(1)\n"
        "v_fma_f32 v42, v38, v248, v249\n"
        "v_fma_f32 v43, v39, v248, v249\n"
        "v_mfma_f32_16x16x32_bf16 v[86:89], v[154:157], v[54:57], v[86:89]\n"
        "ds_read_b64_tr_b16 v[50:51], v4 offset:2848\n"
        "v_fma_f32 v44, v40, v248, v249\n"
        "v_fma_f32 v45, v41, v248, v249\n"
        "v_mfma_f32_16x16x32_bf16 v[90:93], v[158:161], v[54:57], v[90:93]\n"
        "ds_read_b64_tr_b16 v[52:53], v4 offset:2816\n"
        "s_add_u32 s64, s46, 1966080\n"
        "s_addc_u32 s65, s47, 0\n"
        "v_mov_b32 v19, v77\n"
        "v_mfma_f32_16x16x32_bf16 v[78:81], v[138:141], v[58:61], v[78:81]\n"
        "v_add_f32_dpp v22, v97, v64 wave_ror:1 row_mask:0xf bank_mask:0xf\n"
        "v_add_f32_dpp v23, v65, v68 wave_ror:1 row_mask:0xf bank_mask:0xf\n"
        "v_mfma_f32_16x16x32_bf16 v[82:85], v[142:145], v[58:61], v[82:85]\n"
        "v_add_f32_dpp v24, v69, v72 wave_ror:1 row_mask:0xf bank_mask:0xf\n"
        "v_add_f32_dpp v20, v73, v76 wave_ror:1 row_mask:0xf bank_mask:0xf\n"
        "v_mfma_f32_16x16x32_bf16 v[86:89], v[146:149], v[58:61], v[86:89]\n"
        "ds_read_b64_tr_b16 v[54:55], v4 offset:3104\n"
        "v_add_f32_dpp v22, v65, v64 wave_shr:1 row_mask:0xf bank_mask:0xf\n"
        "v_add_f32_dpp v23, v69, v68 wave_shr:1 row_mask:0xf bank_mask:0xf\n"
        "v_mfma_f32_16x16x32_bf16 v[90:93], v[150:153], v[58:61], v[90:93]\n"
        "ds_read_b64_tr_b16 v[56:57], v4 offset:3072\n"
        "v_add_f32_dpp v24, v73, v72 wave_shr:1 row_mask:0xf bank_mask:0xf\n"
        "v_add_f32_dpp v20, v77, v76 wave_shr:1 row_mask:0xf bank_mask:0xf\n"
        "s_waitcnt lgkmcnt(4)\n"
        "v_mfma_f32_16x16x32_bf16 v[78:81], v[130:133], v[46:49], v[78:81]\n"
        "s_setprio 0\n"
        "v_add_f32_dpp v64, v96, v63 wave_ror:1 row_mask:0xf bank_mask:0xf\n"
        "v_add_f32_dpp v68, v22, v67 wave_ror:1 row_mask:0xf bank_mask:0xf\n"
        "v_mfma_f32_16x16x32_bf16 v[82:85], v[134:137], v[46:49], v[82:85]\n"
        "v_add_f32_dpp v72, v23, v71 wave_ror:1 row_mask:0xf bank_mask:0xf\n"
        "v_add_f32_dpp v21, v24, v75 wave_ror:1 row_mask:0xf bank_mask:0xf\n"
        "v_mfma_f32_16x16x32_bf16 v[86:89], v[138:141], v[46:49], v[86:89]\n"
        "ds_read_b64_tr_b16 v[58:59], v4 offset:3360\n"
        "v_add_f32_dpp v64, v22, v63 wave_shr:1 row_mask:0xf bank_mask:0xf\n"
        "v_add_f32_dpp v68, v23, v67 wave_shr:1 row_mask:0xf bank_mask:0xf\n"
        "v_mfma_f32_16x16x32_bf16 v[90:93], v[142:145], v[46:49], v[90:93]\n"
        "ds_read_b64_tr_b16 v[60:61], v4 offset:3328\n"
        "v_add_f32_dpp v72, v24, v71 wave_shr:1 row_mask:0xf bank_mask:0xf\n"
        "v_add_f32_dpp v21, v20, v75 wave_shr:1 row_mask:0xf bank_mask:0xf\n"
        "s_waitcnt vmcnt(1)\n"
        "v_readfirstlane_b32 s52, v226\n"
        "v_readfirstlane_b32 s55, v227\n"
        "s_waitcnt lgkmcnt(4)\n"
        "v_mfma_f32_16x16x32_bf16 v[78:81], v[122:125], v[50:53], v[78:81]\n"
        "v_add_f32_dpp v63, v95, v62 wave_ror:1 row_mask:0xf bank_mask:0xf\n"
        "v_add_f32_dpp v67, v64, v66 wave_ror:1 row_mask:0xf bank_mask:0xf\n"
        "s_bfe_u32 s60, s52, 0x60005\n"
        "s_bitcmp1_b64 s[76:77], s60\n"
        "s_cselect_b32 s59, 1, 0\n"
        "v_mfma_f32_16x16x32_bf16 v[82:85], v[126:129], v[50:53], v[82:85]\n"
        "v_add_f32_dpp v71, v68, v70 wave_ror:1 row_mask:0xf bank_mask:0xf\n"
        "v_add_f32_dpp v75, v72, v74 wave_ror:1 row_mask:0xf bank_mask:0xf\n"
        "v_mfma_f32_16x16x32_bf16 v[86:89], v[130:133], v[50:53], v[86:89]\n"
        "ds_read_b64_tr_b16 v[46:47], v4 offset:3616\n"
        "v_add_f32_dpp v63, v64, v62 wave_shr:1 row_mask:0xf bank_mask:0xf\n"
        "v_add_f32_dpp v67, v68, v66 wave_shr:1 row_mask:0xf bank_mask:0xf\n"
        "v_mfma_f32_16x16x32_bf16 v[90:93], v[134:137], v[50:53], v[90:93]\n"
        "ds_read_b64_tr_b16 v[48:49], v4 offset:3584\n"
        "s_bfe_u32 s60, s52, 0x60015\n"
        "s_bitcmp1_b64 s[76:77], s60\n"
        "s_cselect_b32 s60, 2, 0\n"
        "s_or_b32 s59, s59, s60\n"
        "s_waitcnt lgkmcnt(4)\n"
        "v_mfma_f32_16x16x32_bf16 v[78:81], v[114:117], v[54:57], v[78:81]\n"
        "s_cmp_eq_u32 s55, s31\n"
        "s_cselect_b32 s59, s59, 4\n"
        "ds_write2st64_b32 v5, v63, v67 offset0:8 offset1:9\n"
        "v_add_f32_dpp v71, v72, v70 wave_shr:1 row_mask:0xf bank_mask:0xf\n"
        "v_add_f32_dpp v75, v21, v74 wave_shr:1 row_mask:0xf bank_mask:0xf\n"
        "v_mfma_f32_16x16x32_bf16 v[82:85], v[118:121], v[54:57], v[82:85]\n"
        "ds_write2st64_b32 v5, v71, v75 offset0:10 offset1:11\n"
        "v_mfma_f32_16x16x32_bf16 v[86:89], v[122:125], v[54:57], v[86:89]\n"
        "s_setprio 2\n"
        "ds_read_b64_tr_b16 v[50:51], v4 offset:3872\n"
        "v_mfma_f32_16x16x32_bf16 v[90:93], v[126:129], v[54:57], v[90:93]\n"
        "ds_read_b64_tr_b16 v[52:53], v4 offset:3840\n"
        "s_waitcnt lgkmcnt(6)\n"
        "v_mfma_f32_16x16x32_bf16 v[78:81], v[106:109], v[58:61], v[78:81]\n"
        "v_mfma_f32_16x16x32_bf16 v[82:85], v[110:113], v[58:61], v[82:85]\n"
        "s_waitcnt lgkmcnt(2)\n"
        "s_barrier\n"
        "s_setprio 3\n"
        "ds_read2st64_b32 v[34:35], v6 offset0:8 offset1:24\n"
        "ds_read2st64_b32 v[36:37], v6 offset0:40 offset1:56\n"
        "v_mfma_f32_16x16x32_bf16 v[86:89], v[114:117], v[58:61], v[86:89]\n"
        "v_mfma_f32_16x16x32_bf16 v[90:93], v[118:121], v[58:61], v[90:93]\n"
        "s_setprio 1\n"
        "v_mfma_f32_16x16x32_bf16 v[78:81], v[98:101], v[46:49], v[78:81]\n"
        "v_mfma_f32_16x16x32_bf16 v[82:85], v[102:105], v[46:49], v[82:85]\n"
        "s_waitcnt lgkmcnt(1)\n"
        "v_fmac_f32 v38, v42, v34\n"
        "v_fmac_f32 v39, v43, v35\n"
        "v_mfma_f32_16x16x32_bf16 v[86:89], v[106:109], v[46:49], v[86:89]\n"
        "s_waitcnt lgkmcnt(0)\n"
        "v_fmac_f32 v40, v44, v36\n"
        "v_fmac_f32 v41, v45, v37\n"
        "v_mfma_f32_16x16x32_bf16 v[90:93], v[110:113], v[46:49], v[90:93]\n"
        "s_setprio 0\n"
        "v_cvt_pk_bf16_f32 v34, v38, v39\n"
        "v_cvt_pk_bf16_f32 v35, v40, v41\n"
        "v_mfma_f32_16x16x32_bf16 v[86:89], v[98:101], v[50:53], v[86:89]\n"
        "global_store_dwordx2 v7, v[34:35], s[64:65]\n"
        "ds_read_b64 v[248:249], v235 offset:768\n"
        "v_lshlrev_b32 v38, 16, v242\n"
        "v_mfma_f32_16x16x32_bf16 v[90:93], v[102:105], v[50:53], v[90:93]\n"
        "v_and_b32 v39, 0xffff0000, v242\n"
        "v_lshlrev_b32 v40, 16, v243\n"
        "v_and_b32 v41, 0xffff0000, v243\n"
        "s_waitcnt lgkmcnt(0)\n"
        "v_fma_f32 v42, v38, v248, v249\n"
        "v_fma_f32 v43, v39, v248, v249\n"
        "v_fma_f32 v44, v40, v248, v249\n"
        "v_fma_f32 v45, v41, v248, v249\n"
        "s_add_u32 s64, s46, 2293760\n"
        "s_addc_u32 s65, s47, 0\n"
        "v_add_f32_dpp v22, v19, v80 wave_ror:1 row_mask:0xf bank_mask:0xf\n"
        "v_add_f32_dpp v23, v81, v84 wave_ror:1 row_mask:0xf bank_mask:0xf\n"
        "v_add_f32_dpp v24, v85, v88 wave_ror:1 row_mask:0xf bank_mask:0xf\n"
        "v_add_f32_dpp v17, v89, v92 wave_ror:1 row_mask:0xf bank_mask:0xf\n"
        "v_add_f32_dpp v22, v81, v80 wave_shr:1 row_mask:0xf bank_mask:0xf\n"
        "v_add_f32_dpp v23, v85, v84 wave_shr:1 row_mask:0xf bank_mask:0xf\n"
        "v_add_f32_dpp v24, v89, v88 wave_shr:1 row_mask:0xf bank_mask:0xf\n"
        "v_add_f32_dpp v17, v93, v92 wave_shr:1 row_mask:0xf bank_mask:0xf\n"
        "v_add_f32_dpp v80, v20, v79 wave_ror:1 row_mask:0xf bank_mask:0xf\n"
        "v_add_f32_dpp v84, v22, v83 wave_ror:1 row_mask:0xf bank_mask:0xf\n"
        "v_add_f32_dpp v88, v23, v87 wave_ror:1 row_mask:0xf bank_mask:0xf\n"
        "v_add_f32_dpp v18, v24, v91 wave_ror:1 row_mask:0xf bank_mask:0xf\n"
        "v_add_f32_dpp v80, v22, v79 wave_shr:1 row_mask:0xf bank_mask:0xf\n"
        "v_add_f32_dpp v84, v23, v83 wave_shr:1 row_mask:0xf bank_mask:0xf\n"
        "v_add_f32_dpp v88, v24, v87 wave_shr:1 row_mask:0xf bank_mask:0xf\n"
        "v_add_f32_dpp v18, v17, v91 wave_shr:1 row_mask:0xf bank_mask:0xf\n"
        "v_add_f32_dpp v79, v21, v78 wave_ror:1 row_mask:0xf bank_mask:0xf\n"
        "v_add_f32_dpp v83, v80, v82 wave_ror:1 row_mask:0xf bank_mask:0xf\n"
        "v_add_f32_dpp v87, v84, v86 wave_ror:1 row_mask:0xf bank_mask:0xf\n"
        "v_add_f32_dpp v91, v88, v90 wave_ror:1 row_mask:0xf bank_mask:0xf\n"
        "v_add_f32_dpp v79, v80, v78 wave_shr:1 row_mask:0xf bank_mask:0xf\n"
        "v_add_f32_dpp v83, v84, v82 wave_shr:1 row_mask:0xf bank_mask:0xf\n"
        "ds_write2st64_b32 v5, v79, v83 offset0:12 offset1:13\n"
        "v_add_f32_dpp v87, v88, v86 wave_shr:1 row_mask:0xf bank_mask:0xf\n"
        "v_add_f32_dpp v91, v18, v90 wave_shr:1 row_mask:0xf bank_mask:0xf\n"
        "ds_write2st64_b32 v5, v87, v91 offset0:14 offset1:15\n"
        "s_and_b32 s55, s59, 3\n"
        "s_cbranch_scc0 .LconvL_ne_h3\n"
        "s_bitcmp1_b32 s59, 0\n"
        "s_cselect_b32 s60, 0, 16\n"
        "s_cselect_b32 s61, 0, 5120\n"
        "s_add_u32 s66, s14, s61\n"
        "s_addc_u32 s67, s15, 0\n"
        "s_lshr_b32 s60, s52, s60\n"
        "s_and_b32 s60, s60, 0x7ff\n"
        "s_lshl_b32 s62, s60, 3\n"
        "s_add_u32 s68, s48, s62\n"
        "s_addc_u32 s69, s49, 0\n"
        "s_mul_i32 s63, s60, 1280\n"
        "v_lshl_add_u32 v170, v232, 2, s63\n"
        "s_lshr_b32 s61, s60, 8\n"
        "s_lshl_b32 s61, s61, 2\n"
        "s_lshr_b32 s61, s74, s61\n"
        "s_and_b32 s61, s61, 15\n"
        "s_lshl_b32 s61, s61, 8\n"
        "s_and_b32 s62, s60, 255\n"
        "s_add_u32 s61, s61, s62\n"
        "s_lshl_b32 s61, s61, 2\n"
        "v_add_u32 v195, s61, v251\n"
        "global_load_dwordx4 v[178:181], v228, s[66:67] sc1\n"
        "global_load_dwordx4 v[182:185], v229, s[66:67] sc1\n"
        "global_load_dwordx4 v[186:189], v230, s[66:67] sc1\n"
        "global_load_dwordx4 v[190:193], v231, s[66:67] sc1\n"
        "global_load_dwordx2 v[174:175], v13, s[68:69] sc1\n"
        "global_load_dword v171, v170, s[44:45]\n"
        ".LconvL_ne_h3:\n"
        "s_waitcnt lgkmcnt(0)\n"
        "s_barrier\n"
        "ds_read2st64_b32 v[34:35], v6 offset0:12 offset1:28\n"
        "ds_read2st64_b32 v[36:37], v6 offset0:44 offset1:60\n"
        "s_waitcnt lgkmcnt(1)\n"
        "v_fmac_f32 v38, v42, v34\n"
        "v_fmac_f32 v39, v43, v35\n"
        "s_waitcnt lgkmcnt(0)\n"
        "v_fmac_f32 v40, v44, v36\n"
        "v_fmac_f32 v41, v45, v37\n"
        "v_cvt_pk_bf16_f32 v34, v38, v39\n"
        "v_cvt_pk_bf16_f32 v35, v40, v41\n"
        "global_store_dwordx2 v7, v[34:35], s[64:65]\n"
        "s_waitcnt vmcnt(1)\n"
        ".Lrouted_h3:\n"
        "s_cmp_eq_u32 s59, 0\n"
        "s_cbranch_scc1 .Lrt_done_h3\n"
        "s_cmp_lg_u32 s59, 4\n"
        "s_cbranch_scc1 .Lrt_fin_h3\n"
        "v_cmp_ne_u32 vcc, s31, v227\n"
        "s_nop 0\n"
        "s_cbranch_vccnz .Lrsel_p_h3\n"
        ".Lrsel_r_h3:\n"
        "v_readfirstlane_b32 s52, v226\n"
        "s_nop 0\n"
        "s_bfe_u32 s60, s52, 0x60005\n"
        "s_bitcmp1_b64 s[76:77], s60\n"
        "s_cselect_b32 s59, 1, 0\n"
        "s_bfe_u32 s60, s52, 0x60015\n"
        "s_bitcmp1_b64 s[76:77], s60\n"
        "s_cselect_b32 s60, 2, 0\n"
        "s_or_b32 s59, s59, s60\n"
        "s_cbranch_scc0 .Lrt_done_h3\n"
        ".Lrt_again_h3:\n"
        "s_bitcmp1_b32 s59, 0\n"
        "s_cselect_b32 s60, 0, 16\n"
        "s_cselect_b32 s61, 0, 5120\n"
        "s_add_u32 s66, s14, s61\n"
        "s_addc_u32 s67, s15, 0\n"
        "s_lshr_b32 s60, s52, s60\n"
        "s_and_b32 s60, s60, 0x7ff\n"
        "s_lshl_b32 s62, s60, 3\n"
        "s_add_u32 s68, s48, s62\n"
        "s_addc_u32 s69, s49, 0\n"
        "s_mul_i32 s63, s60, 1280\n"
        "v_lshl_add_u32 v170, v232, 2, s63\n"
        "s_lshr_b32 s61, s60, 8\n"
        "s_lshl_b32 s61, s61, 2\n"
        "s_lshr_b32 s61, s74, s61\n"
        "s_and_b32 s61, s61, 15\n"
        "s_lshl_b32 s61, s61, 8\n"
        "s_and_b32 s62, s60, 255\n"
        "s_add_u32 s61, s61, s62\n"
        "s_lshl_b32 s61, s61, 2\n"
        "v_add_u32 v195, s61, v251\n"
        "global_load_dwordx4 v[178:181], v228, s[66:67] sc1\n"
        "global_load_dwordx4 v[182:185], v229, s[66:67] sc1\n"
        "global_load_dwordx4 v[186:189], v230, s[66:67] sc1\n"
        "global_load_dwordx4 v[190:193], v231, s[66:67] sc1\n"
        "global_load_dwordx2 v[174:175], v13, s[68:69] sc1\n"
        "global_load_dword v171, v170, s[44:45]\n"
        "s_waitcnt vmcnt(0)\n"
        ".Lrt_fin_h3:\n"
        "ds_read2st64_b32 v[172:173], v195 offset1:16\n"
        "v_min3_u32 v176, v175, v179, v181\n"
        "v_min3_u32 v176, v176, v183, v185\n"
        "v_min3_u32 v176, v176, v187, v189\n"
        "v_min3_u32 v176, v176, v191, v193\n"
        "v_max3_u32 v177, v175, v179, v181\n"
        "v_max3_u32 v177, v177, v183, v185\n"
        "v_max3_u32 v177, v177, v187, v189\n"
        "v_max3_u32 v177, v177, v191, v193\n"
        "v_cmp_ne_u32 vcc, s31, v176\n"
        "v_cmp_ne_u32 s[84:85], s31, v177\n"
        "s_nop 0\n"
        "s_or_b64 vcc, vcc, s[84:85]\n"
        "s_cbranch_vccnz .Lrt_p_h3\n"
        ".Lrt_r_h3:\n"
        "v_cndmask_b32 v190, 0, v190, s[80:81]\n"
        "v_cndmask_b32 v192, 0, v192, s[80:81]\n"
        "v_add_f32 v178, v178, v182\n"
        "v_add_f32 v180, v180, v184\n"
        "v_add_f32 v186, v186, v190\n"
        "v_add_f32 v188, v188, v192\n"
        "v_add_f32 v178, v178, v186\n"
        "v_add_f32 v180, v180, v188\n"
        "v_and_b32 v175, 0xffff0000, v174\n"
        "v_rcp_f32 v175, v175\n"
        "v_permlane32_swap_b32 v178, v180\n"
        "v_add_f32 v178, v178, v180\n"
        "v_lshlrev_b32 v174, 16, v174\n"
        "v_lshlrev_b32 v176, 16, v171\n"
        "v_add_f32_dpp v178, v178, v178 row_ror:8 row_mask:0xf bank_mask:0xf bound_ctrl:1\n"
        "v_and_b32 v177, 0xffff0000, v171\n"
        "v_sub_f32 v182, v176, v174\n"
        "v_add_f32_dpp v178, v178, v178 row_ror:4 row_mask:0xf bank_mask:0xf bound_ctrl:1\n"
        "v_mov_b32 v179, v178\n"
        "v_sub_f32 v183, v177, v174\n"
        "v_mul_f32 v182, v182, v175\n"
        "v_permlane16_swap_b32 v178, v179\n"
        "v_add_f32 v178, v178, v179\n"
        "v_mov_b32 v179, v178\n"
        "v_mul_f32 v183, v183, v175\n"
        "s_waitcnt lgkmcnt(0)\n"
        "v_fmac_f32 v176, v182, v172\n"
        "v_permlane32_swap_b32 v178, v179\n"
        "v_fmac_f32 v177, v183, v173\n"
        "v_add_f32 v176, v176, v178\n"
        "v_add_f32 v177, v177, v179\n"
        "v_cvt_pk_bf16_f32 v176, v176, v177\n"
        "s_mov_b64 exec, 15\n"
        "global_store_dword v170, v176, s[46:47]\n"
        "s_mov_b64 exec, -1\n"
        "s_cmp_eq_u32 s59, 3\n"
        "s_mov_b32 s59, 2\n"
        "s_cbranch_scc1 .Lrt_again_h3\n"
        ".Lrt_done_h3:\n"
        "s_endpgm\n"
        ".Lspare_h3:\n"
        "s_lshl_b32 s53, s29, 3\n"
        "s_mov_b32 s54, 0x6040200\n"
        "s_mov_b32 s55, 0x17120d08\n"
        "s_lshr_b64 s[54:55], s[54:55], s53\n"
        "s_and_b32 s89, s54, 0xff\n"
        "s_sub_u32 s87, s30, s56\n"
        "s_lshr_b32 s34, 0x8aaa4444, s52\n"
        "s_and_b32 s34, s34, 15\n"
        "s_lshl_b32 s33, s29, 4\n"
        "s_sub_u32 s33, s33, s88\n"
        "s_lshl1_add_u32 s33, s33, s87\n"
        "v_lshlrev_b32 v2, 4, v1\n"
        "v_lshlrev_b32 v3, 2, v1\n"
        "s_mul_i32 s52, s28, 160\n"
        "s_add_u32 m0, s52, 131072\n"
        "v_add_u32 v4, s52, v2\n"
        "s_add_u32 s36, s33, 96\n"
        "s_lshl1_add_u32 s53, s89, s34\n"
        "s_sub_u32 s53, s33, s53\n"
        "s_lshl1_add_u32 s46, s89, s87\n"
        "s_cmp_ge_u32 s87, s34\n"
        "s_cselect_b32 s46, s53, s46\n"
        "s_cselect_b32 s53, 0, 42\n"
        "s_cselect_b32 s54, 63, 22\n"
        "s_cselect_b32 s55, 0, 0\n"
        "s_add_u32 s37, s46, s53\n"
        "s_add_u32 s37, s37, 192\n"
        "s_add_u32 s38, s46, 234\n"
        "s_cmp_lt_u32 s46, 22\n"
        "s_cselect_b32 s39, -1, 0\n"
        "s_lshl3_add_u32 s40, s33, s28\n"
        "s_mul_i32 s40, s40, 1280\n"
        "s_lshl3_add_u32 s42, s36, s28\n"
        "s_mul_i32 s42, s42, 1280\n"
        "s_lshl3_add_u32 s44, s37, s28\n"
        "s_mul_i32 s44, s44, 1280\n"
        "s_lshl3_add_u32 s68, s38, s28\n"
        "s_mul_i32 s68, s68, 1280\n"
        "s_waitcnt lgkmcnt(0)\n"
        "s_mov_b64 exec, 0x3ff\n"
        "global_load_lds_dwordx4 v4, s[10:11]\n"
        "s_mov_b64 exec, -1\n"
        "s_load_dword s31, s[14:15], 0x0\n"
        "s_add_u32 s40, s8, s40\n"
        "s_addc_u32 s41, s9, 0\n"
        "s_add_u32 s42, s8, s42\n"
        "s_addc_u32 s43, s9, 0\n"
        "s_add_u32 s44, s8, s44\n"
        "s_addc_u32 s45, s9, 0\n"
        "s_add_u32 s68, s8, s68\n"
        "s_addc_u32 s69, s9, 0\n"
        "s_sleep 12\n"
        "s_cmp_ge_u32 s87, s34\n"
        "s_cbranch_scc1 .Lnosl_h3\n"
        "s_sleep 2\n"
        ".Lnosl_h3:\n"
        "s_cmp_ge_u32 s46, s54\n"
        "s_cbranch_scc1 .Lnorow2_h3\n"
        "global_load_dwordx4 v[20:23], v2, s[44:45]\n"
        "global_load_dword v24, v3, s[44:45] offset:1024\n"
        ".Lnorow2_h3:\n"
        "global_load_dwordx4 v[8:11], v2, s[40:41]\n"
        "global_load_dword v12, v3, s[40:41] offset:1024\n"
        "global_load_dwordx4 v[14:17], v2, s[42:43]\n"
        "global_load_dword v18, v3, s[42:43] offset:1024\n"
        "s_cmp_ge_u32 s87, s34\n"
        "s_cbranch_scc1 .Lhelper_h3\n"
        "s_and_b32 s52, s28, 3\n"
        "s_mul_i32 s53, s52, 552960\n"
        "s_lshl_b32 s54, s46, 6\n"
        "s_add_u32 s53, s53, s54\n"
        "s_cmp_ge_u32 s28, 4\n"
        "s_cselect_b32 s56, s22, s20\n"
        "s_cselect_b32 s57, s23, s21\n"
        "s_add_u32 s56, s56, s53\n"
        "s_addc_u32 s57, s57, 0\n"
        "s_bitcmp1_b32 s46, 0\n"
        "s_cselect_b32 s53, 331776, 0\n"
        "s_cselect_b32 s54, 0, 221184\n"
        "s_cselect_b32 s62, 192, 0\n"
        "s_mul_i32 s55, s28, 10240\n"
        "v_lshrrev_b32 v130, 2, v1\n"
        "v_mul_u32_u24 v130, 3456, v130\n"
        "v_and_b32 v56, 3, v1\n"
        "v_and_b32 v57, 32, v1\n"
        "v_lshlrev_b32 v58, 4, v56\n"
        "v_xor_b32 v58, v58, v57\n"
        "v_add_u32 v130, v130, v58\n"
        "s_mul_i32 s47, s46, 40960\n"
        "s_mul_i32 s48, s28, 5120\n"
        "s_add_u32 s47, s47, s48\n"
        "s_add_u32 s48, s48, 81920\n"
        "s_lshl_b32 s49, s28, 6\n"
        "s_and_b32 s49, s49, 128\n"
        "v_xor_b32 v140, s49, v2\n"
        "s_add_u32 s58, s56, s53\n"
        "s_addc_u32 s59, s57, 0\n"
        "s_add_u32 s60, s56, s54\n"
        "s_addc_u32 s61, s57, 0\n"
        "s_add_u32 s50, s24, s47\n"
        "s_addc_u32 s51, s25, 0\n"
        "v_xor_b32 v141, 0, v140\n"
        "v_xor_b32 v144, 32, v140\n"
        "v_cmp_gt_u32 vcc, 16, v1\n"
        "s_nop 1\n"
        "v_cndmask_b32 v141, v144, v141, vcc\n"
        "v_xor_b32 v142, 32, v140\n"
        "v_xor_b32 v144, 64, v140\n"
        "v_cmp_gt_u32 vcc, 32, v1\n"
        "s_nop 1\n"
        "v_cndmask_b32 v142, v144, v142, vcc\n"
        "v_xor_b32 v143, 64, v140\n"
        "v_xor_b32 v144, 96, v140\n"
        "v_cmp_gt_u32 vcc, 48, v1\n"
        "s_nop 1\n"
        "v_cndmask_b32 v143, v144, v143, vcc\n"
        "v_add_u32 v144, 4096, v144\n"
        "v_and_b32 v134, 15, v1\n"
        "v_lshrrev_b32 v135, 4, v1\n"
        "s_mul_i32 s64, s29, 1536\n"
        "s_add_u32 s64, s64, 24576\n"
        "v_lshl_add_u32 v131, v1, 3, s64\n"
        "v_mov_b32 v132, 0\n"
        "v_mov_b32 v133, 0\n"
        "s_mul_i32 s53, s52, 320\n"
        "s_add_u32 s53, s53, 133120\n"
        "v_bfe_u32 v57, v1, 2, 1\n"
        "v_mul_u32_u24 v57, 1344, v57\n"
        "v_lshl_add_u32 v57, v135, 4, v57\n"
        "v_add_u32 v57, s53, v57\n"
        "v_add_u32 v128, s62, v57\n"
        "s_cmp_eq_u32 s62, 0\n"
        "s_cselect_b32 s53, 128, 0\n"
        "v_add_u32 v129, s53, v57\n"
        "v_cmp_gt_u32 s[78:79], 16, v1\n"
        "v_add_u32 v57, 1024, v2\n"
        "v_subrev_u32 v58, 320, v2\n"
        "v_cndmask_b32 v145, v58, v57, s[78:79]\n"
        "v_lshrrev_b32 v127, 5, v1\n"
        "v_lshlrev_b32 v127, 4, v127\n"
        "v_add_u32 v127, 136000, v127\n"
        "v_and_b32 v138, 16, v1\n"
        "v_add_u32 v138, 136000, v138\n"
        "v_lshrrev_b32 v57, 2, v134\n"
        "v_lshl_add_u32 v60, v135, 3, v57\n"
        "v_and_b32 v61, 1, v135\n"
        "v_cmp_eq_u32 s[80:81], 0, v61\n"
        "v_lshlrev_b32 v116, 6, v60\n"
        "v_lshl_add_u32 v116, v56, 3, v116\n"
        "v_lshl_add_u32 v116, v61, 5, v116\n"
        "v_add_u32 v116, s55, v116\n"
        "v_xor_b32 v117, 32, v116\n"
        "v_mul_u32_u24 v60, 1280, v60\n"
        "v_lshl_add_u32 v60, v56, 3, v60\n"
        "v_add_u32 v60, 81920, v60\n"
        "v_lshl_add_u32 v57, v61, 2, v57\n"
        "v_lshlrev_b32 v57, 5, v57\n"
        "s_mul_i32 s53, s28, 160\n"
        "s_add_u32 s54, s53, 0\n"
        "v_xor_b32 v118, s54, v57\n"
        "v_add_u32 v118, v118, v60\n"
        "s_add_u32 s54, s53, 32\n"
        "v_xor_b32 v119, s54, v57\n"
        "v_add_u32 v119, v119, v60\n"
        "s_add_u32 s54, s53, 64\n"
        "v_xor_b32 v120, s54, v57\n"
        "v_add_u32 v120, v120, v60\n"
        "s_add_u32 s54, s53, 96\n"
        "v_xor_b32 v121, s54, v57\n"
        "v_add_u32 v121, v121, v60\n"
        "s_add_u32 s54, s53, 128\n"
        "v_xor_b32 v122, s54, v57\n"
        "v_add_u32 v122, v122, v60\n"
        "v_mul_u32_u24 v123, 640, v61\n"
        "v_lshrrev_b32 v57, 1, v135\n"
        "v_lshl_add_u32 v123, v57, 4, v123\n"
        "v_add_u32 v123, v123, v134\n"
        "s_mul_i32 s53, s46, 10240\n"
        "s_mul_i32 s54, s28, 640\n"
        "s_add_u32 s53, s53, s54\n"
        "s_add_u32 s53, s53, 65536\n"
        "v_lshl_add_u32 v123, v123, 3, s53\n"
        "s_lshl_b32 s53, s28, 2\n"
        "s_add_u32 s53, s53, 122880\n"
        "v_mul_u32_u24 v125, 48, v1\n"
        "v_add_u32 v124, s53, v125\n"
        "v_add_u32 v125, 122880, v125\n"
        "s_lshl_b32 s53, s28, 7\n"
        "s_add_u32 s53, s53, 128000\n"
        "v_lshl_add_u32 v136, v61, 1, v57\n"
        "v_lshl_add_u32 v136, v136, 4, v134\n"
        "v_lshl_add_u32 v136, v136, 1, s53\n"
        "v_bfe_u32 v137, v1, 2, 1\n"
        "v_lshlrev_b32 v137, 6, v137\n"
        "v_lshl_add_u32 v137, v135, 4, v137\n"
        "v_add_u32 v137, s53, v137\n"
        "s_lshl_b32 s53, s46, 7\n"
        "s_add_u32 s53, s53, 40960\n"
        "v_lshl_add_u32 v139, v1, 3, s53\n"
        "v_cmp_gt_u32 s[82:83], 32, v1\n"
        "s_movk_i32 s77, 1280\n"
        "s_add_u32 s66, s14, 4096\n"
        "s_addc_u32 s67, s15, 0\n"
        "v_and_b32 v54, 7, v1\n"
        "v_lshlrev_b32 v49, 2, v54\n"
        "v_add_u32 v49, 158720, v49\n"
        "v_and_b32 v51, 1, v1\n"
        "v_lshrrev_b32 v50, 1, v1\n"
        "v_mul_u32_u24 v50, 1536, v50\n"
        "v_lshl_add_u32 v50, v51, 3, v50\n"
        "s_lshl_b32 s52, s33, 4\n"
        "s_add_u32 s52, s52, 24576\n"
        "v_add_u32 v50, s52, v50\n"
        "v_bfe_u32 v55, v1, 3, 2\n"
        "v_lshl_add_u32 v52, v55, 6, v49\n"
        "v_lshrrev_b32 v6, 4, v1\n"
        "v_mov_b32 v7, s33\n"
        "v_mov_b32 v53, s33\n"
        "v_mov_b32 v4, s36\n"
        "v_cmp_eq_u32 vcc, 1, v6\n"
        "s_nop 1\n"
        "v_cndmask_b32 v7, v7, v4, vcc\n"
        "v_cmp_eq_u32 vcc, 1, v55\n"
        "s_nop 1\n"
        "v_cndmask_b32 v53, v53, v4, vcc\n"
        "v_mov_b32 v4, s37\n"
        "v_cmp_eq_u32 vcc, 2, v6\n"
        "s_nop 1\n"
        "v_cndmask_b32 v7, v7, v4, vcc\n"
        "v_cmp_eq_u32 vcc, 2, v55\n"
        "s_nop 1\n"
        "v_cndmask_b32 v53, v53, v4, vcc\n"
        "v_lshl_add_u32 v53, v53, 3, v54\n"
        "v_lshlrev_b32 v53, 3, v53\n"
        "v_add_u32 v53, 4096, v53\n"
        "v_lshl_add_u32 v7, v7, 3, s28\n"
        "v_sub_u32 v7, 2047, v7\n"
        "v_and_b32 v7, 0x7ff, v7\n"
        "s_lshl_b32 s53, s28, 2\n"
        "s_add_u32 s53, s53, 158720\n"
        "v_lshl_add_u32 v31, v6, 6, s53\n"
        "v_add_u32 v4, 131072, v2\n"
        "v_add_u32 v5, 132096, v3\n"
        "v_mov_b32 v13, 0\n"
        "v_mov_b32 v32, 0\n"
        "v_mov_b32 v33, 0\n"
        "v_mov_b32 v34, 0\n"
        "v_mov_b32 v35, 0\n"
        "v_mov_b32 v36, 0\n"
        "v_mov_b32 v37, 0\n"
        "v_mov_b32 v38, 0\n"
        "v_mov_b32 v39, 0\n"
        "v_mov_b32 v40, 0\n"
        "v_mov_b32 v41, 0\n"
        "v_mov_b32 v42, 0\n"
        "v_mov_b32 v43, 0\n"
        "s_waitcnt vmcnt(4)\n"
        "s_barrier\n"
        "ds_read_b128 v[26:29], v4\n"
        "ds_read_b32 v30, v5\n"
        "s_mov_b32 m0, s48\n"
        "s_nop 0\n"
        "global_load_lds_dwordx4 v140, s[50:51] offset:0\n"
        "s_mov_b32 m0, s48\n"
        "s_nop 0\n"
        "global_load_lds_dwordx4 v141, s[50:51] offset:1024\n"
        "s_waitcnt lgkmcnt(0)\n"
        "s_add_u32 s31, s31, 0x9E3779B9\n"
        "v_dot2c_f32_bf16 v13, 0x3f803f80, v26\n"
        "v_dot2c_f32_bf16 v13, 0x3f803f80, v27\n"
        "v_dot2c_f32_bf16 v13, 0x3f803f80, v28\n"
        "v_dot2c_f32_bf16 v13, 0x3f803f80, v29\n"
        "v_dot2c_f32_bf16 v13, 0x3f803f80, v30\n"
        "s_nop 2\n"
        "v_add_f32_dpp v13, v13, v13 row_ror:8 row_mask:0xf bank_mask:0xf bound_ctrl:1\n"
        "s_nop 1\n"
        "v_add_f32_dpp v13, v13, v13 row_ror:4 row_mask:0xf bank_mask:0xf bound_ctrl:1\n"
        "s_nop 1\n"
        "v_add_f32_dpp v13, v13, v13 row_ror:2 row_mask:0xf bank_mask:0xf bound_ctrl:1\n"
        "s_nop 1\n"
        "v_add_f32_dpp v13, v13, v13 row_ror:1 row_mask:0xf bank_mask:0xf bound_ctrl:1\n"
        "v_mov_b32 v19, v13\n"
        "s_nop 1\n"
        "v_permlane16_swap_b32 v13, v19\n"
        "v_add_f32 v13, v13, v19\n"
        "v_mov_b32 v19, v13\n"
        "s_nop 1\n"
        "v_permlane32_swap_b32 v13, v19\n"
        "v_add_f32 v13, v13, v19\n"
        "v_dot2c_f32_bf16 v34, 0x3f803f80, v20\n"
        "v_dot2c_f32_bf16 v38, v20, v20\n"
        "v_dot2c_f32_bf16 v42, v20, v26\n"
        "v_dot2c_f32_bf16 v34, 0x3f803f80, v21\n"
        "v_dot2c_f32_bf16 v38, v21, v21\n"
        "v_dot2c_f32_bf16 v42, v21, v27\n"
        "v_dot2c_f32_bf16 v34, 0x3f803f80, v22\n"
        "v_dot2c_f32_bf16 v38, v22, v22\n"
        "v_dot2c_f32_bf16 v42, v22, v28\n"
        "v_dot2c_f32_bf16 v34, 0x3f803f80, v23\n"
        "v_dot2c_f32_bf16 v38, v23, v23\n"
        "v_dot2c_f32_bf16 v42, v23, v29\n"
        "v_dot2c_f32_bf16 v34, 0x3f803f80, v24\n"
        "v_dot2c_f32_bf16 v38, v24, v24\n"
        "v_dot2c_f32_bf16 v42, v24, v30\n"
        "s_waitcnt vmcnt(5)\n"
        "v_dot2c_f32_bf16 v32, 0x3f803f80, v8\n"
        "v_dot2c_f32_bf16 v36, v8, v8\n"
        "v_dot2c_f32_bf16 v40, v8, v26\n"
        "v_dot2c_f32_bf16 v32, 0x3f803f80, v9\n"
        "v_dot2c_f32_bf16 v36, v9, v9\n"
        "v_dot2c_f32_bf16 v40, v9, v27\n"
        "v_dot2c_f32_bf16 v32, 0x3f803f80, v10\n"
        "v_dot2c_f32_bf16 v36, v10, v10\n"
        "v_dot2c_f32_bf16 v40, v10, v28\n"
        "v_dot2c_f32_bf16 v32, 0x3f803f80, v11\n"
        "v_dot2c_f32_bf16 v36, v11, v11\n"
        "v_dot2c_f32_bf16 v40, v11, v29\n"
        "s_waitcnt vmcnt(4)\n"
        "v_dot2c_f32_bf16 v32, 0x3f803f80, v12\n"
        "v_dot2c_f32_bf16 v36, v12, v12\n"
        "v_dot2c_f32_bf16 v40, v12, v30\n"
        "s_nop 0\n"
        "v_permlane32_swap_b32 v32, v34\n"
        "v_permlane32_swap_b32 v36, v38\n"
        "v_permlane32_swap_b32 v40, v42\n"
        "v_add_f32 v32, v32, v34\n"
        "v_add_f32 v36, v36, v38\n"
        "v_add_f32 v40, v40, v42\n"
        "s_waitcnt vmcnt(3)\n"
        "v_dot2c_f32_bf16 v33, 0x3f803f80, v14\n"
        "v_dot2c_f32_bf16 v37, v14, v14\n"
        "v_dot2c_f32_bf16 v41, v14, v26\n"
        "v_dot2c_f32_bf16 v33, 0x3f803f80, v15\n"
        "v_dot2c_f32_bf16 v37, v15, v15\n"
        "v_dot2c_f32_bf16 v41, v15, v27\n"
        "v_dot2c_f32_bf16 v33, 0x3f803f80, v16\n"
        "v_dot2c_f32_bf16 v37, v16, v16\n"
        "v_dot2c_f32_bf16 v41, v16, v28\n"
        "v_dot2c_f32_bf16 v33, 0x3f803f80, v17\n"
        "v_dot2c_f32_bf16 v37, v17, v17\n"
        "v_dot2c_f32_bf16 v41, v17, v29\n"
        "s_waitcnt vmcnt(2)\n"
        "v_dot2c_f32_bf16 v33, 0x3f803f80, v18\n"
        "v_dot2c_f32_bf16 v37, v18, v18\n"
        "v_dot2c_f32_bf16 v41, v18, v30\n"
        "s_nop 0\n"
        "v_permlane32_swap_b32 v33, v35\n"
        "v_permlane32_swap_b32 v37, v39\n"
        "v_permlane32_swap_b32 v41, v43\n"
        "v_add_f32 v33, v33, v35\n"
        "v_add_f32 v37, v37, v39\n"
        "v_add_f32 v41, v41, v43\n"
        "v_permlane16_swap_b32 v32, v33\n"
        "v_permlane16_swap_b32 v36, v37\n"
        "v_permlane16_swap_b32 v40, v41\n"
        "v_add_f32 v32, v32, v33\n"
        "v_add_f32 v36, v36, v37\n"
        "v_add_f32 v40, v40, v41\n"
        "v_add_f32_dpp v32, v32, v32 row_ror:8 row_mask:0xf bank_mask:0xf bound_ctrl:1\n"
        "v_add_f32_dpp v36, v36, v36 row_ror:8 row_mask:0xf bank_mask:0xf bound_ctrl:1\n"
        "v_add_f32_dpp v40, v40, v40 row_ror:8 row_mask:0xf bank_mask:0xf bound_ctrl:1\n"
        "v_add_f32_dpp v32, v32, v32 row_ror:4 row_mask:0xf bank_mask:0xf bound_ctrl:1\n"
        "v_add_f32_dpp v36, v36, v36 row_ror:4 row_mask:0xf bank_mask:0xf bound_ctrl:1\n"
        "v_add_f32_dpp v40, v40, v40 row_ror:4 row_mask:0xf bank_mask:0xf bound_ctrl:1\n"
        "v_add_f32_dpp v32, v32, v32 row_ror:2 row_mask:0xf bank_mask:0xf bound_ctrl:1\n"
        "v_add_f32_dpp v36, v36, v36 row_ror:2 row_mask:0xf bank_mask:0xf bound_ctrl:1\n"
        "v_add_f32_dpp v40, v40, v40 row_ror:2 row_mask:0xf bank_mask:0xf bound_ctrl:1\n"
        "v_add_f32_dpp v32, v32, v32 row_ror:1 row_mask:0xf bank_mask:0xf bound_ctrl:1\n"
        "v_add_f32_dpp v36, v36, v36 row_ror:1 row_mask:0xf bank_mask:0xf bound_ctrl:1\n"
        "v_add_f32_dpp v40, v40, v40 row_ror:1 row_mask:0xf bank_mask:0xf bound_ctrl:1\n"
        "v_mul_f32 v32, 0x3acccccd, v32\n"
        "v_mul_f32 v36, 0x3acccccd, v36\n"
        "v_fma_f32 v36, -v32, v32, v36\n"
        "v_max_f32 v36, 0, v36\n"
        "v_cvt_pk_bf16_f32 v36, 0, v36\n"
        "v_add_f32 v36, 0x3727c5ac, v36\n"
        "v_cvt_pk_bf16_f32 v36, 0, v36\n"
        "v_sqrt_f32 v36, v36\n"
        "v_cvt_pk_bf16_f32 v8, 0, v32\n"
        "v_cvt_pk_bf16_f32 v32, v32, v36\n"
        "v_and_b32 v36, 0xffff0000, v32\n"
        "v_rcp_f32 v36, v36\n"
        "v_fma_f32 v40, -v8, v13, v40\n"
        "v_mul_f32 v40, v40, v36\n"
        "v_cvt_pk_bf16_f32 v40, 0, v40\n"
        "v_ashrrev_i32 v8, 31, v40\n"
        "v_or_b32 v8, 0x80000000, v8\n"
        "v_xor_b32 v40, v40, v8\n"
        "s_mov_b32 exec_lo, 0x00010001\n"
        "s_mov_b32 exec_hi, 0x00010001\n"
        "v_lshrrev_b32 v40, 16, v40\n"
        "v_lshl_or_b32 v40, v40, 11, v7\n"
        "ds_write2_b32 v31, v32, v40 offset1:8\n"
        "s_mov_b64 exec, -1\n"
        "s_waitcnt lgkmcnt(0)\n"
        "s_barrier\n"
        "s_cmp_gt_u32 s28, 1\n"
        "s_cbranch_scc1 .Lpub_xF_h3\n"
        "s_cmp_eq_u32 s28, 1\n"
        "s_cbranch_scc1 .Lpub_sF_h3\n"
        "ds_read2_b32 v[12:13], v49 offset0:8 offset1:24\n"
        "ds_read2_b32 v[14:15], v49 offset0:40 offset1:56\n"
        "v_mov_b32 v11, s31\n"
        "v_cmp_eq_u32 vcc, 0, v51\n"
        "s_waitcnt lgkmcnt(0)\n"
        "v_and_b32 v14, s39, v14\n"
        "v_max3_u32 v10, v12, v13, v14\n"
        "v_med3_u32 v12, v12, v13, v14\n"
        "s_nop 0\n"
        "v_min_u32_dpp v13, v10, v10 row_ror:4 row_mask:0xf bank_mask:0xf bound_ctrl:1\n"
        "v_max_u32_dpp v10, v10, v10 row_ror:4 row_mask:0xf bank_mask:0xf bound_ctrl:1\n"
        "v_max_u32_dpp v12, v12, v12 row_ror:4 row_mask:0xf bank_mask:0xf bound_ctrl:1\n"
        "v_max_u32 v12, v12, v13\n"
        "v_min_u32_dpp v13, v10, v10 row_ror:2 row_mask:0xf bank_mask:0xf bound_ctrl:1\n"
        "v_max_u32_dpp v10, v10, v10 row_ror:2 row_mask:0xf bank_mask:0xf bound_ctrl:1\n"
        "v_max_u32_dpp v12, v12, v12 row_ror:2 row_mask:0xf bank_mask:0xf bound_ctrl:1\n"
        "v_max_u32 v12, v12, v13\n"
        "v_min_u32_dpp v13, v10, v10 row_ror:1 row_mask:0xf bank_mask:0xf bound_ctrl:1\n"
        "v_max_u32_dpp v10, v10, v10 row_ror:1 row_mask:0xf bank_mask:0xf bound_ctrl:1\n"
        "v_max_u32_dpp v12, v12, v12 row_ror:1 row_mask:0xf bank_mask:0xf bound_ctrl:1\n"
        "v_max_u32 v12, v12, v13\n"
        "v_cndmask_b32 v10, v12, v10, vcc\n"
        "s_mov_b64 exec, 0xffff\n"
        "global_atomic_swap_x2 v50, v[10:11], s[14:15] sc1\n"
        "s_mov_b64 exec, -1\n"
        "s_branch .Lpub_xF_h3\n"
        ".Lpub_sF_h3:\n"
        "ds_read_b32 v10, v52\n"
        "v_mov_b32 v11, s31\n"
        "s_and_b32 s52, s39, 0xff0000\n"
        "s_or_b32 exec_lo, s52, 0xffff\n"
        "s_mov_b32 exec_hi, 0\n"
        "s_waitcnt lgkmcnt(0)\n"
        "global_store_dwordx2 v53, v[10:11], s[14:15] sc1\n"
        "s_mov_b64 exec, -1\n"
        ".Lpub_xF_h3:\n"
        "s_barrier\n"
        "v_mov_b32 v149, s31\n"
        "v_mov_b32 v151, s31\n"
        "v_mov_b32 v153, s31\n"
        "s_cmp_lg_u32 s28, 0\n"
        "s_cbranch_scc1 .LffB_h3\n"
        "s_mov_b32 m0, s48\n"
        "s_nop 0\n"
        "global_load_lds_dwordx4 v142, s[50:51] offset:2048\n"
        "s_mov_b32 m0, s48\n"
        "s_nop 0\n"
        "global_load_lds_dwordx4 v143, s[50:51] offset:3072\n"
        "s_add_u32 m0, s48, 4096\n"
        "s_nop 0\n"
        "global_load_lds_dwordx4 v144, s[50:51]\n"
        "s_add_u32 m0, s55, 0\n"
        "s_nop 0\n"
        "global_load_lds_dwordx4 v130, s[58:59]\n"
        "s_add_u32 s58, s58, 55296\n"
        "s_addc_u32 s59, s59, 0\n"
        "v_mov_b32 v24, 0x3f803f80\n"
        "v_mov_b32 v25, 0x3f803f80\n"
        "v_mov_b32 v26, 0x3f803f80\n"
        "v_mov_b32 v27, 0x3f803f80\n"
        "v_mov_b32 v28, 0x3f803f80\n"
        "v_mov_b32 v29, 0x3f803f80\n"
        "v_mov_b32 v30, 0x3f803f80\n"
        "v_mov_b32 v31, 0x3f803f80\n"
        "v_mov_b32 v32, 0x3f803f80\n"
        "v_mov_b32 v33, 0x3f803f80\n"
        "v_mov_b32 v34, 0x3f803f80\n"
        "v_mov_b32 v35, 0x3f803f80\n"
        "v_mov_b32 v36, 0x3f803f80\n"
        "v_mov_b32 v37, 0x3f803f80\n"
        "v_mov_b32 v38, 0x3f803f80\n"
        "v_mov_b32 v39, 0x3f803f80\n"
        "v_mov_b32 v40, 0x3f803f80\n"
        "v_mov_b32 v41, 0x3f803f80\n"
        "v_mov_b32 v42, 0x3f803f80\n"
        "v_mov_b32 v43, 0x3f803f80\n"
        "s_waitcnt vmcnt(1)\n"
        "global_atomic_or_x2 v[8:9], v131, v[132:133], s[14:15] offset:0 sc0 sc1\n"
        "global_atomic_or_x2 v[10:11], v131, v[132:133], s[14:15] offset:512 sc0 sc1\n"
        "global_atomic_or_x2 v[12:13], v131, v[132:133], s[14:15] offset:1024 sc0 sc1\n"
        "s_barrier\n"
        "ds_read_b64_tr_b16 v[88:89], v118\n"
        "ds_read_b64_tr_b16 v[90:91], v118 offset:5120\n"
        "ds_read_b64_tr_b16 v[92:93], v119\n"
        "ds_read_b64_tr_b16 v[94:95], v119 offset:5120\n"
        "ds_read_b64_tr_b16 v[96:97], v120\n"
        "ds_read_b64_tr_b16 v[98:99], v120 offset:5120\n"
        "ds_read_b64_tr_b16 v[100:101], v121\n"
        "ds_read_b64_tr_b16 v[102:103], v121 offset:5120\n"
        "ds_read_b64_tr_b16 v[104:105], v122\n"
        "ds_read_b64_tr_b16 v[106:107], v122 offset:5120\n"
        "s_add_u32 m0, s55, 1024\n"
        "s_nop 0\n"
        "global_load_lds_dwordx4 v130, s[58:59]\n"
        "s_add_u32 s58, s58, 55296\n"
        "s_addc_u32 s59, s59, 0\n"
        "s_waitcnt vmcnt(3)\n"
        "v_cmp_ne_u32 vcc, s31, v9\n"
        "s_waitcnt vmcnt(2)\n"
        "v_cmp_ne_u32 s[84:85], s31, v11\n"
        "s_nop 0\n"
        "s_or_b64 vcc, vcc, s[84:85]\n"
        "s_waitcnt vmcnt(1)\n"
        "v_cmp_ne_u32 s[84:85], s31, v13\n"
        "s_nop 0\n"
        "s_or_b64 vcc, vcc, s[84:85]\n"
        "s_cbranch_vccnz .Lkeys_p_h3\n"
        ".Lkeys_r_h3:\n"
        "v_max3_u32 v20, v8, v10, v12\n"
        "v_med3_u32 v21, v8, v10, v12\n"
        "s_nop 0\n"
        "v_min_u32_dpp v19, v20, v20 row_ror:8 row_mask:0xf bank_mask:0xf bound_ctrl:1\n"
        "v_max_u32_dpp v20, v20, v20 row_ror:8 row_mask:0xf bank_mask:0xf bound_ctrl:1\n"
        "v_max_u32_dpp v21, v21, v21 row_ror:8 row_mask:0xf bank_mask:0xf bound_ctrl:1\n"
        "v_max_u32 v21, v21, v19\n"
        "v_min_u32_dpp v19, v20, v20 row_ror:4 row_mask:0xf bank_mask:0xf bound_ctrl:1\n"
        "v_max_u32_dpp v20, v20, v20 row_ror:4 row_mask:0xf bank_mask:0xf bound_ctrl:1\n"
        "v_max_u32_dpp v21, v21, v21 row_ror:4 row_mask:0xf bank_mask:0xf bound_ctrl:1\n"
        "v_max_u32 v21, v21, v19\n"
        "v_min_u32_dpp v19, v20, v20 row_ror:2 row_mask:0xf bank_mask:0xf bound_ctrl:1\n"
        "v_max_u32_dpp v20, v20, v20 row_ror:2 row_mask:0xf bank_mask:0xf bound_ctrl:1\n"
        "v_max_u32_dpp v21, v21, v21 row_ror:2 row_mask:0xf bank_mask:0xf bound_ctrl:1\n"
        "v_max_u32 v21, v21, v19\n"
        "v_min_u32_dpp v19, v20, v20 row_ror:1 row_mask:0xf bank_mask:0xf bound_ctrl:1\n"
        "v_max_u32_dpp v20, v20, v20 row_ror:1 row_mask:0xf bank_mask:0xf bound_ctrl:1\n"
        "v_max_u32_dpp v21, v21, v21 row_ror:1 row_mask:0xf bank_mask:0xf bound_ctrl:1\n"
        "v_max_u32 v21, v21, v19\n"
        "v_mov_b32 v8, v20\n"
        "v_mov_b32 v9, v21\n"
        "s_nop 0\n"
        "v_permlane16_swap_b32 v20, v8\n"
        "v_permlane16_swap_b32 v21, v9\n"
        "v_min_u32 v19, v20, v8\n"
        "v_max_u32 v20, v20, v8\n"
        "v_max3_u32 v21, v21, v9, v19\n"
        "v_mov_b32 v8, v20\n"
        "v_mov_b32 v9, v21\n"
        "s_nop 0\n"
        "v_permlane32_swap_b32 v20, v8\n"
        "v_permlane32_swap_b32 v21, v9\n"
        "v_min_u32 v19, v20, v8\n"
        "v_max_u32 v20, v20, v8\n"
        "v_max3_u32 v21, v21, v9, v19\n"
        "v_not_b32 v10, v20\n"
        "v_not_b32 v11, v21\n"
        "v_and_b32 v10, 0x7ff, v10\n"
        "v_and_b32 v11, 0x7ff, v11\n"
        "v_mad_u32_u24 v8, v10, s77, v2\n"
        "s_mov_b32 m0, 133120\n"
        "v_cndmask_b32 v18, v11, v10, s[78:79]\n"
        "global_load_lds_dwordx4 v8, s[8:9]\n"
        "v_mad_u32_u24 v9, v18, s77, v145\n"
        "s_mov_b32 m0, 134144\n"
        "v_mad_u32_u24 v12, v11, s77, v2\n"
        "s_mov_b32 exec_lo, 0xfff0ffff\n"
        "global_load_lds_dwordx4 v9, s[8:9]\n"
        "s_mov_b32 m0, 134464\n"
        "s_mov_b32 exec_lo, -1\n"
        "s_mov_b32 exec_hi, 15\n"
        "global_load_lds_dwordx4 v12, s[8:9] offset:704\n"
        "s_mov_b64 exec, -1\n"
        "v_cndmask_b32 v18, v11, v10, s[82:83]\n"
        "v_lshlrev_b32 v18, 3, v18\n"
        "global_load_dwordx2 v[16:17], v18, s[66:67] sc1\n"
        "v_lshl_or_b32 v10, v11, 16, v10\n"
        "v_mov_b32 v11, s31\n"
        "s_mov_b64 exec, 0xffff\n"
        "global_store_dwordx2 v139, v[10:11], s[14:15] sc1\n"
        "s_mov_b64 exec, -1\n"
        "v_cndmask_b32 v14, v21, v20, s[82:83]\n"
        "v_lshrrev_b32 v14, 11, v14\n"
        "v_lshlrev_b32 v14, 16, v14\n"
        "v_ashrrev_i32 v15, 31, v14\n"
        "v_not_b32 v15, v15\n"
        "v_and_b32 v15, 0x7fff0000, v15\n"
        "v_xor_b32 v14, v14, v15\n"
        "v_mul_f32 v14, 0x3fb8aa3b, v14\n"
        "v_exp_f32 v14, v14\n"
        "s_nop 0\n"
        "v_add_f32 v14, 1.0, v14\n"
        "v_rcp_f32 v14, v14\n"
        "s_nop 0\n"
        "v_cvt_pk_bf16_f32 v14, 0, v14\n"
        "s_add_u32 m0, s55, 2048\n"
        "s_nop 0\n"
        "global_load_lds_dwordx4 v130, s[58:59]\n"
        "s_add_u32 s58, s58, 55296\n"
        "s_addc_u32 s59, s59, 0\n"
        "s_add_u32 m0, s55, 3072\n"
        "s_nop 0\n"
        "global_load_lds_dwordx4 v130, s[58:59]\n"
        "s_add_u32 m0, s55, 4096\n"
        "s_nop 0\n"
        "global_load_lds_dwordx4 v130, s[60:61]\n"
        "s_add_u32 s60, s60, 55296\n"
        "s_addc_u32 s61, s61, 0\n"
        "s_add_u32 m0, s55, 5120\n"
        "s_nop 0\n"
        "global_load_lds_dwordx4 v130, s[60:61]\n"
        "s_add_u32 s60, s60, 55296\n"
        "s_addc_u32 s61, s61, 0\n"
        "s_waitcnt vmcnt(9)\n"
        "ds_read_b64_tr_b16 v[48:49], v116 offset:0\n"
        "ds_read_b64_tr_b16 v[50:51], v116 offset:256\n"
        "ds_read_b64_tr_b16 v[52:53], v117 offset:0\n"
        "ds_read_b64_tr_b16 v[54:55], v117 offset:256\n"
        "s_add_u32 m0, s55, 6144\n"
        "s_nop 0\n"
        "global_load_lds_dwordx4 v130, s[60:61]\n"
        "s_add_u32 s60, s60, 55296\n"
        "s_addc_u32 s61, s61, 0\n"
        "s_add_u32 m0, s55, 7168\n"
        "s_nop 0\n"
        "global_load_lds_dwordx4 v130, s[60:61]\n"
        "s_add_u32 s60, s60, 55296\n"
        "s_addc_u32 s61, s61, 0\n"
        "s_waitcnt vmcnt(4)\n"
        "ds_read_b64_tr_b16 v[56:57], v116 offset:2048\n"
        "ds_read_b64_tr_b16 v[58:59], v116 offset:2304\n"
        "ds_read_b64_tr_b16 v[60:61], v117 offset:2048\n"
        "ds_read_b64_tr_b16 v[62:63], v117 offset:2304\n"
        "s_add_u32 m0, s55, 8192\n"
        "s_nop 0\n"
        "global_load_lds_dwordx4 v130, s[60:61]\n"
        "s_add_u32 s60, s60, 55296\n"
        "s_addc_u32 s61, s61, 0\n"
        "s_add_u32 m0, s55, 9216\n"
        "s_nop 0\n"
        "global_load_lds_dwordx4 v130, s[60:61]\n"
        "s_waitcnt vmcnt(4)\n"
        "ds_read_b64_tr_b16 v[64:65], v116 offset:4096\n"
        "ds_read_b64_tr_b16 v[66:67], v116 offset:4352\n"
        "ds_read_b64_tr_b16 v[68:69], v117 offset:4096\n"
        "ds_read_b64_tr_b16 v[70:71], v117 offset:4352\n"
        "s_waitcnt vmcnt(2)\n"
        "ds_read_b64_tr_b16 v[72:73], v116 offset:6144\n"
        "ds_read_b64_tr_b16 v[74:75], v116 offset:6400\n"
        "ds_read_b64_tr_b16 v[76:77], v117 offset:6144\n"
        "ds_read_b64_tr_b16 v[78:79], v117 offset:6400\n"
        "v_cmp_ne_u32 vcc, s31, v17\n"
        "s_nop 0\n"
        "s_cbranch_vccnz .Lfst_p_h3\n"
        ".Lfst_r_h3:\n"
        "v_and_b32 v13, 0xffff0000, v16\n"
        "v_rcp_f32 v13, v13\n"
        "v_lshlrev_b32 v12, 16, v16\n"
        "s_mov_b32 exec_lo, 1\n"
        "s_mov_b32 exec_hi, 1\n"
        "ds_write_b96 v127, v[12:14]\n"
        "s_mov_b64 exec, -1\n"
        "s_waitcnt lgkmcnt(0)\n"
        "s_branch .Lffj_h3\n"
        ".LffB_h3:\n"
        "s_mov_b32 m0, s48\n"
        "s_nop 0\n"
        "global_load_lds_dwordx4 v142, s[50:51] offset:2048\n"
        "s_mov_b32 m0, s48\n"
        "s_nop 0\n"
        "global_load_lds_dwordx4 v143, s[50:51] offset:3072\n"
        "s_add_u32 m0, s48, 4096\n"
        "s_nop 0\n"
        "global_load_lds_dwordx4 v144, s[50:51]\n"
        "s_add_u32 m0, s55, 0\n"
        "s_nop 0\n"
        "global_load_lds_dwordx4 v130, s[58:59]\n"
        "s_add_u32 s58, s58, 55296\n"
        "s_addc_u32 s59, s59, 0\n"
        "v_mov_b32 v24, 0x3f803f80\n"
        "v_mov_b32 v25, 0x3f803f80\n"
        "v_mov_b32 v26, 0x3f803f80\n"
        "v_mov_b32 v27, 0x3f803f80\n"
        "v_mov_b32 v28, 0x3f803f80\n"
        "v_mov_b32 v29, 0x3f803f80\n"
        "v_mov_b32 v30, 0x3f803f80\n"
        "v_mov_b32 v31, 0x3f803f80\n"
        "v_mov_b32 v32, 0x3f803f80\n"
        "v_mov_b32 v33, 0x3f803f80\n"
        "v_mov_b32 v34, 0x3f803f80\n"
        "v_mov_b32 v35, 0x3f803f80\n"
        "v_mov_b32 v36, 0x3f803f80\n"
        "v_mov_b32 v37, 0x3f803f80\n"
        "v_mov_b32 v38, 0x3f803f80\n"
        "v_mov_b32 v39, 0x3f803f80\n"
        "v_mov_b32 v40, 0x3f803f80\n"
        "v_mov_b32 v41, 0x3f803f80\n"
        "v_mov_b32 v42, 0x3f803f80\n"
        "v_mov_b32 v43, 0x3f803f80\n"
        "s_waitcnt vmcnt(1)\n"
        "s_add_u32 m0, s55, 1024\n"
        "s_nop 0\n"
        "global_load_lds_dwordx4 v130, s[58:59]\n"
        "s_add_u32 s58, s58, 55296\n"
        "s_addc_u32 s59, s59, 0\n"
        "s_barrier\n"
        "ds_read_b64_tr_b16 v[88:89], v118\n"
        "ds_read_b64_tr_b16 v[90:91], v118 offset:5120\n"
        "ds_read_b64_tr_b16 v[92:93], v119\n"
        "ds_read_b64_tr_b16 v[94:95], v119 offset:5120\n"
        "ds_read_b64_tr_b16 v[96:97], v120\n"
        "ds_read_b64_tr_b16 v[98:99], v120 offset:5120\n"
        "ds_read_b64_tr_b16 v[100:101], v121\n"
        "ds_read_b64_tr_b16 v[102:103], v121 offset:5120\n"
        "ds_read_b64_tr_b16 v[104:105], v122\n"
        "ds_read_b64_tr_b16 v[106:107], v122 offset:5120\n"
        "s_add_u32 m0, s55, 2048\n"
        "s_nop 0\n"
        "global_load_lds_dwordx4 v130, s[58:59]\n"
        "s_add_u32 s58, s58, 55296\n"
        "s_addc_u32 s59, s59, 0\n"
        "s_add_u32 m0, s55, 3072\n"
        "s_nop 0\n"
        "global_load_lds_dwordx4 v130, s[58:59]\n"
        "s_waitcnt vmcnt(2)\n"
        "ds_read_b64_tr_b16 v[48:49], v116 offset:0\n"
        "ds_read_b64_tr_b16 v[50:51], v116 offset:256\n"
        "ds_read_b64_tr_b16 v[52:53], v117 offset:0\n"
        "ds_read_b64_tr_b16 v[54:55], v117 offset:256\n"
        "s_add_u32 m0, s55, 4096\n"
        "s_nop 0\n"
        "global_load_lds_dwordx4 v130, s[60:61]\n"
        "s_add_u32 s60, s60, 55296\n"
        "s_addc_u32 s61, s61, 0\n"
        "s_add_u32 m0, s55, 5120\n"
        "s_nop 0\n"
        "global_load_lds_dwordx4 v130, s[60:61]\n"
        "s_add_u32 s60, s60, 55296\n"
        "s_addc_u32 s61, s61, 0\n"
        "s_waitcnt vmcnt(2)\n"
        "ds_read_b64_tr_b16 v[56:57], v116 offset:2048\n"
        "ds_read_b64_tr_b16 v[58:59], v116 offset:2304\n"
        "ds_read_b64_tr_b16 v[60:61], v117 offset:2048\n"
        "ds_read_b64_tr_b16 v[62:63], v117 offset:2304\n"
        "s_add_u32 m0, s55, 6144\n"
        "s_nop 0\n"
        "global_load_lds_dwordx4 v130, s[60:61]\n"
        "s_add_u32 s60, s60, 55296\n"
        "s_addc_u32 s61, s61, 0\n"
        "s_add_u32 m0, s55, 7168\n"
        "s_nop 0\n"
        "global_load_lds_dwordx4 v130, s[60:61]\n"
        "s_add_u32 s60, s60, 55296\n"
        "s_addc_u32 s61, s61, 0\n"
        "s_waitcnt vmcnt(2)\n"
        "ds_read_b64_tr_b16 v[64:65], v116 offset:4096\n"
        "ds_read_b64_tr_b16 v[66:67], v116 offset:4352\n"
        "ds_read_b64_tr_b16 v[68:69], v117 offset:4096\n"
        "ds_read_b64_tr_b16 v[70:71], v117 offset:4352\n"
        "s_add_u32 m0, s55, 8192\n"
        "s_nop 0\n"
        "global_load_lds_dwordx4 v130, s[60:61]\n"
        "s_add_u32 s60, s60, 55296\n"
        "s_addc_u32 s61, s61, 0\n"
        "s_add_u32 m0, s55, 9216\n"
        "s_nop 0\n"
        "global_load_lds_dwordx4 v130, s[60:61]\n"
        "s_waitcnt vmcnt(2)\n"
        "ds_read_b64_tr_b16 v[72:73], v116 offset:6144\n"
        "ds_read_b64_tr_b16 v[74:75], v116 offset:6400\n"
        "ds_read_b64_tr_b16 v[76:77], v117 offset:6144\n"
        "ds_read_b64_tr_b16 v[78:79], v117 offset:6400\n"
        ".Lffj_h3:\n"
        "s_barrier\n"
        "s_mov_b32 exec_lo, 0x11111111\n"
        "s_mov_b32 exec_hi, 0x11111111\n"
        "ds_read_b128 v[24:27], v128 offset:0\n"
        "ds_read_b128 v[28:31], v128 offset:64\n"
        "ds_read_b128 v[32:35], v129 offset:0\n"
        "ds_read_b128 v[36:39], v129 offset:64\n"
        "ds_read_b128 v[40:43], v129 offset:128\n"
        "s_mov_b64 exec, -1\n"
        "ds_read_b128 v[8:11], v138\n"
        "s_waitcnt lgkmcnt(5)\n"
        "v_mfma_f32_16x16x32_bf16 v[108:111], v[24:27], v[48:51], 0\n"
        "v_mfma_f32_16x16x32_bf16 v[112:115], v[24:27], v[52:55], 0\n"
        "s_waitcnt lgkmcnt(4)\n"
        "v_mfma_f32_16x16x32_bf16 v[108:111], v[28:31], v[56:59], v[108:111]\n"
        "v_mfma_f32_16x16x32_bf16 v[112:115], v[28:31], v[60:63], v[112:115]\n"
        "s_waitcnt lgkmcnt(3)\n"
        "v_mfma_f32_16x16x32_bf16 v[108:111], v[32:35], v[64:67], v[108:111]\n"
        "v_mfma_f32_16x16x32_bf16 v[112:115], v[32:35], v[68:71], v[112:115]\n"
        "s_waitcnt lgkmcnt(2)\n"
        "v_mfma_f32_16x16x32_bf16 v[108:111], v[36:39], v[72:75], v[108:111]\n"
        "v_mfma_f32_16x16x32_bf16 v[112:115], v[36:39], v[76:79], v[112:115]\n"
        "s_waitcnt vmcnt(0)\n"
        "ds_read_b64_tr_b16 v[80:81], v116 offset:8192\n"
        "ds_read_b64_tr_b16 v[82:83], v116 offset:8448\n"
        "ds_read_b64_tr_b16 v[84:85], v117 offset:8192\n"
        "ds_read_b64_tr_b16 v[86:87], v117 offset:8448\n"
        "s_waitcnt lgkmcnt(2)\n"
        "v_mfma_f32_16x16x32_bf16 v[108:111], v[40:43], v[80:83], v[108:111]\n"
        "s_waitcnt lgkmcnt(0)\n"
        "v_mfma_f32_16x16x32_bf16 v[112:115], v[40:43], v[84:87], v[112:115]\n"
        "v_mov_b32 v13, v10\n"
        "s_nop 4\n"
        "v_fma_f32 v108, -v8, v109, v108\n"
        "s_nop 0\n"
        "v_fma_f32 v112, -v8, v113, v112\n"
        "v_cndmask_b32 v108, v112, v108, s[82:83]\n"
        "ds_write_b32 v124, v108\n"
        "s_waitcnt lgkmcnt(0)\n"
        "s_barrier\n"
        "ds_read_b128 v[24:27], v125\n"
        "ds_read_b128 v[28:31], v125 offset:16\n"
        "s_waitcnt lgkmcnt(1)\n"
        "v_add_f32 v24, v24, v25\n"
        "v_add_f32 v26, v26, v27\n"
        "v_add_f32 v24, v24, v26\n"
        "v_mul_f32 v24, v24, v9\n"
        "v_cvt_pk_bf16_f32 v24, 0, v24\n"
        "v_mul_f32 v25, 0xbfb8aa3b, v24\n"
        "v_exp_f32 v25, v25\n"
        "s_waitcnt lgkmcnt(0)\n"
        "v_add_f32 v28, v28, v29\n"
        "v_add_f32 v25, 1.0, v25\n"
        "v_rcp_f32 v25, v25\n"
        "v_add_f32 v30, v30, v31\n"
        "v_add_f32 v28, v28, v30\n"
        "v_mul_f32 v24, v24, v25\n"
        "v_mul_f32 v28, v28, v9\n"
        "v_cvt_pk_bf16_f32 v24, 0, v24\n"
        "v_cvt_pk_bf16_f32 v28, 0, v28\n"
        "v_mul_f32 v24, v24, v28\n"
        "v_cvt_pk_bf16_f32 v24, v24, v24\n"
        "ds_write_b16 v136, v24\n"
        "ds_read_b128 v[24:27], v137\n"
        "s_waitcnt lgkmcnt(0)\n"
        "v_mfma_f32_16x16x32_bf16 v[48:51], v[24:27], v[88:91], 0\n"
        "v_mfma_f32_16x16x32_bf16 v[52:55], v[24:27], v[92:95], 0\n"
        "v_mfma_f32_16x16x32_bf16 v[56:59], v[24:27], v[96:99], 0\n"
        "v_mfma_f32_16x16x32_bf16 v[60:63], v[24:27], v[100:103], 0\n"
        "v_mfma_f32_16x16x32_bf16 v[64:67], v[24:27], v[104:107], 0\n"
        "s_nop 4\n"
        "v_cndmask_b32 v148, v52, v48, s[82:83]\n"
        "v_mul_f32 v148, v148, v13\n"
        "global_store_dwordx2 v123, v[148:149], s[14:15] sc1\n"
        "v_cndmask_b32 v150, v60, v56, s[82:83]\n"
        "v_mul_f32 v150, v150, v13\n"
        "global_store_dwordx2 v123, v[150:151], s[14:15] offset:256 sc1\n"
        "v_mul_f32 v152, v64, v13\n"
        "s_mov_b32 exec_hi, 0\n"
        "global_store_dwordx2 v123, v[152:153], s[14:15] offset:512 sc1\n"
        "s_mov_b64 exec, -1\n"
        "s_endpgm\n"
        ".Lhelper_h3:\n"
        "s_sub_u32 s47, 16, s35\n"
        "s_lshl_b32 s47, s47, 1\n"
        "s_sub_u32 s47, s47, s34\n"
        "s_lshl_b32 s47, s47, 3\n"
        "s_sub_u32 s48, s87, s34\n"
        "s_lshl3_add_u32 s48, s48, s28\n"
        "s_lshl_b32 s50, s34, 2\n"
        "s_mul_i32 s49, s50, 8\n"
        "v_cvt_f32_u32 v44, s50\n"
        "v_rcp_f32 v44, v44\n"
        "s_nop 0\n"
        "v_mul_f32 v44, 0x47800000, v44\n"
        "v_cvt_u32_f32 v44, v44\n"
        "s_nop 0\n"
        "v_readfirstlane_b32 s51, v44\n"
        "v_mul_u32_u24 v44, 3456, v1\n"
        "v_lshrrev_b32 v45, 3, v1\n"
        "v_mul_u32_u24 v45, 5120, v45\n"
        "v_and_b32 v46, 7, v1\n"
        "v_lshl_add_u32 v45, v46, 7, v45\n"
        "s_add_u32 s51, s51, 1\n"
        "s_mov_b32 s60, 0x71825493\n"
        "s_mov_b32 s61, 0x0\n"
        "s_mov_b32 m0, 159488\n"
        "s_sub_u32 s52, s49, 1\n"
        "s_min_u32 s52, s52, s48\n"
        "s_mul_i32 s54, s52, s51\n"
        "s_lshr_b32 s54, s54, 16\n"
        "s_mul_i32 s55, s54, s50\n"
        "s_sub_u32 s52, s52, s55\n"
        "s_lshl_b32 s54, s54, 2\n"
        "s_lshr_b64 s[54:55], s[60:61], s54\n"
        "s_and_b32 s54, s54, 15\n"
        "s_add_u32 s48, s48, s47\n"
        "s_cmp_ge_u32 s54, 10\n"
        "s_cbranch_scc1 .Ljd0_h3\n"
        "s_bfe_u32 s53, s52, 0x20001\n"
        "s_mul_i32 s53, s53, 10\n"
        "s_add_u32 s54, s54, s53\n"
        "s_mul_i32 s54, s54, 55296\n"
        "s_lshr_b32 s53, s52, 3\n"
        "s_add_u32 s53, s53, s89\n"
        "s_lshl_b32 s53, s53, 7\n"
        "s_add_u32 s53, s53, s54\n"
        "s_bitcmp1_b32 s52, 0\n"
        "s_cselect_b32 s70, s22, s20\n"
        "s_cselect_b32 s71, s23, s21\n"
        "v_mov_b32 v60, v44\n"
        "s_branch .Ljx0_h3\n"
        ".Ljd0_h3:\n"
        "s_lshl3_add_u32 s53, s89, s52\n"
        "s_mul_i32 s53, s53, 10\n"
        "s_add_u32 s53, s53, s54\n"
        "s_sub_u32 s53, s53, 10\n"
        "s_lshl_b32 s53, s53, 10\n"
        "s_mov_b64 s[70:71], s[24:25]\n"
        "v_mov_b32 v60, v45\n"
        ".Ljx0_h3:\n"
        "s_add_u32 s70, s70, s53\n"
        "s_addc_u32 s71, s71, 0\n"
        "v_and_b32 v54, 7, v1\n"
        "v_lshlrev_b32 v49, 2, v54\n"
        "v_add_u32 v49, 158720, v49\n"
        "v_and_b32 v51, 1, v1\n"
        "v_lshrrev_b32 v50, 1, v1\n"
        "v_mul_u32_u24 v50, 1536, v50\n"
        "v_lshl_add_u32 v50, v51, 3, v50\n"
        "s_lshl_b32 s52, s33, 4\n"
        "s_add_u32 s52, s52, 24576\n"
        "v_add_u32 v50, s52, v50\n"
        "v_bfe_u32 v55, v1, 3, 2\n"
        "v_lshl_add_u32 v52, v55, 6, v49\n"
        "v_lshrrev_b32 v6, 4, v1\n"
        "v_mov_b32 v7, s33\n"
        "v_mov_b32 v53, s33\n"
        "v_mov_b32 v4, s36\n"
        "v_cmp_eq_u32 vcc, 1, v6\n"
        "s_nop 1\n"
        "v_cndmask_b32 v7, v7, v4, vcc\n"
        "v_cmp_eq_u32 vcc, 1, v55\n"
        "s_nop 1\n"
        "v_cndmask_b32 v53, v53, v4, vcc\n"
        "v_mov_b32 v4, s37\n"
        "v_cmp_eq_u32 vcc, 2, v6\n"
        "s_nop 1\n"
        "v_cndmask_b32 v7, v7, v4, vcc\n"
        "v_cmp_eq_u32 vcc, 2, v55\n"
        "s_nop 1\n"
        "v_cndmask_b32 v53, v53, v4, vcc\n"
        "v_lshl_add_u32 v53, v53, 3, v54\n"
        "v_lshlrev_b32 v53, 3, v53\n"
        "v_add_u32 v53, 4096, v53\n"
        "v_lshl_add_u32 v7, v7, 3, s28\n"
        "v_sub_u32 v7, 2047, v7\n"
        "v_and_b32 v7, 0x7ff, v7\n"
        "s_lshl_b32 s53, s28, 2\n"
        "s_add_u32 s53, s53, 158720\n"
        "v_lshl_add_u32 v31, v6, 6, s53\n"
        "v_add_u32 v4, 131072, v2\n"
        "v_add_u32 v5, 132096, v3\n"
        "v_mov_b32 v13, 0\n"
        "v_mov_b32 v32, 0\n"
        "v_mov_b32 v33, 0\n"
        "v_mov_b32 v34, 0\n"
        "v_mov_b32 v35, 0\n"
        "v_mov_b32 v36, 0\n"
        "v_mov_b32 v37, 0\n"
        "v_mov_b32 v38, 0\n"
        "v_mov_b32 v39, 0\n"
        "v_mov_b32 v40, 0\n"
        "v_mov_b32 v41, 0\n"
        "v_mov_b32 v42, 0\n"
        "v_mov_b32 v43, 0\n"
        "s_waitcnt vmcnt(4)\n"
        "s_barrier\n"
        "ds_read_b128 v[26:29], v4\n"
        "ds_read_b32 v30, v5\n"
        "s_mov_b64 exec, 0xffff\n"
        "global_load_lds_dword v60, s[70:71]\n"
        "s_mov_b64 exec, -1\n"
        "s_waitcnt lgkmcnt(0)\n"
        "s_add_u32 s31, s31, 0x9E3779B9\n"
        "v_dot2c_f32_bf16 v13, 0x3f803f80, v26\n"
        "v_dot2c_f32_bf16 v13, 0x3f803f80, v27\n"
        "v_dot2c_f32_bf16 v13, 0x3f803f80, v28\n"
        "v_dot2c_f32_bf16 v13, 0x3f803f80, v29\n"
        "v_dot2c_f32_bf16 v13, 0x3f803f80, v30\n"
        "s_nop 2\n"
        "v_add_f32_dpp v13, v13, v13 row_ror:8 row_mask:0xf bank_mask:0xf bound_ctrl:1\n"
        "s_nop 1\n"
        "v_add_f32_dpp v13, v13, v13 row_ror:4 row_mask:0xf bank_mask:0xf bound_ctrl:1\n"
        "s_nop 1\n"
        "v_add_f32_dpp v13, v13, v13 row_ror:2 row_mask:0xf bank_mask:0xf bound_ctrl:1\n"
        "s_nop 1\n"
        "v_add_f32_dpp v13, v13, v13 row_ror:1 row_mask:0xf bank_mask:0xf bound_ctrl:1\n"
        "v_mov_b32 v19, v13\n"
        "s_nop 1\n"
        "v_permlane16_swap_b32 v13, v19\n"
        "v_add_f32 v13, v13, v19\n"
        "v_mov_b32 v19, v13\n"
        "s_nop 1\n"
        "v_permlane32_swap_b32 v13, v19\n"
        "v_add_f32 v13, v13, v19\n"
        "v_dot2c_f32_bf16 v34, 0x3f803f80, v20\n"
        "v_dot2c_f32_bf16 v38, v20, v20\n"
        "v_dot2c_f32_bf16 v42, v20, v26\n"
        "v_dot2c_f32_bf16 v34, 0x3f803f80, v21\n"
        "v_dot2c_f32_bf16 v38, v21, v21\n"
        "v_dot2c_f32_bf16 v42, v21, v27\n"
        "v_dot2c_f32_bf16 v34, 0x3f803f80, v22\n"
        "v_dot2c_f32_bf16 v38, v22, v22\n"
        "v_dot2c_f32_bf16 v42, v22, v28\n"
        "v_dot2c_f32_bf16 v34, 0x3f803f80, v23\n"
        "v_dot2c_f32_bf16 v38, v23, v23\n"
        "v_dot2c_f32_bf16 v42, v23, v29\n"
        "v_dot2c_f32_bf16 v34, 0x3f803f80, v24\n"
        "v_dot2c_f32_bf16 v38, v24, v24\n"
        "v_dot2c_f32_bf16 v42, v24, v30\n"
        "s_waitcnt vmcnt(4)\n"
        "v_dot2c_f32_bf16 v32, 0x3f803f80, v8\n"
        "v_dot2c_f32_bf16 v36, v8, v8\n"
        "v_dot2c_f32_bf16 v40, v8, v26\n"
        "v_dot2c_f32_bf16 v32, 0x3f803f80, v9\n"
        "v_dot2c_f32_bf16 v36, v9, v9\n"
        "v_dot2c_f32_bf16 v40, v9, v27\n"
        "v_dot2c_f32_bf16 v32, 0x3f803f80, v10\n"
        "v_dot2c_f32_bf16 v36, v10, v10\n"
        "v_dot2c_f32_bf16 v40, v10, v28\n"
        "v_dot2c_f32_bf16 v32, 0x3f803f80, v11\n"
        "v_dot2c_f32_bf16 v36, v11, v11\n"
        "v_dot2c_f32_bf16 v40, v11, v29\n"
        "s_waitcnt vmcnt(3)\n"
        "v_dot2c_f32_bf16 v32, 0x3f803f80, v12\n"
        "v_dot2c_f32_bf16 v36, v12, v12\n"
        "v_dot2c_f32_bf16 v40, v12, v30\n"
        "s_nop 0\n"
        "v_permlane32_swap_b32 v32, v34\n"
        "v_permlane32_swap_b32 v36, v38\n"
        "v_permlane32_swap_b32 v40, v42\n"
        "v_add_f32 v32, v32, v34\n"
        "v_add_f32 v36, v36, v38\n"
        "v_add_f32 v40, v40, v42\n"
        "s_waitcnt vmcnt(2)\n"
        "v_dot2c_f32_bf16 v33, 0x3f803f80, v14\n"
        "v_dot2c_f32_bf16 v37, v14, v14\n"
        "v_dot2c_f32_bf16 v41, v14, v26\n"
        "v_dot2c_f32_bf16 v33, 0x3f803f80, v15\n"
        "v_dot2c_f32_bf16 v37, v15, v15\n"
        "v_dot2c_f32_bf16 v41, v15, v27\n"
        "v_dot2c_f32_bf16 v33, 0x3f803f80, v16\n"
        "v_dot2c_f32_bf16 v37, v16, v16\n"
        "v_dot2c_f32_bf16 v41, v16, v28\n"
        "v_dot2c_f32_bf16 v33, 0x3f803f80, v17\n"
        "v_dot2c_f32_bf16 v37, v17, v17\n"
        "v_dot2c_f32_bf16 v41, v17, v29\n"
        "s_waitcnt vmcnt(1)\n"
        "v_dot2c_f32_bf16 v33, 0x3f803f80, v18\n"
        "v_dot2c_f32_bf16 v37, v18, v18\n"
        "v_dot2c_f32_bf16 v41, v18, v30\n"
        "s_nop 0\n"
        "v_permlane32_swap_b32 v33, v35\n"
        "v_permlane32_swap_b32 v37, v39\n"
        "v_permlane32_swap_b32 v41, v43\n"
        "v_add_f32 v33, v33, v35\n"
        "v_add_f32 v37, v37, v39\n"
        "v_add_f32 v41, v41, v43\n"
        "v_permlane16_swap_b32 v32, v33\n"
        "v_permlane16_swap_b32 v36, v37\n"
        "v_permlane16_swap_b32 v40, v41\n"
        "v_add_f32 v32, v32, v33\n"
        "v_add_f32 v36, v36, v37\n"
        "v_add_f32 v40, v40, v41\n"
        "v_add_f32_dpp v32, v32, v32 row_ror:8 row_mask:0xf bank_mask:0xf bound_ctrl:1\n"
        "v_add_f32_dpp v36, v36, v36 row_ror:8 row_mask:0xf bank_mask:0xf bound_ctrl:1\n"
        "v_add_f32_dpp v40, v40, v40 row_ror:8 row_mask:0xf bank_mask:0xf bound_ctrl:1\n"
        "v_add_f32_dpp v32, v32, v32 row_ror:4 row_mask:0xf bank_mask:0xf bound_ctrl:1\n"
        "v_add_f32_dpp v36, v36, v36 row_ror:4 row_mask:0xf bank_mask:0xf bound_ctrl:1\n"
        "v_add_f32_dpp v40, v40, v40 row_ror:4 row_mask:0xf bank_mask:0xf bound_ctrl:1\n"
        "v_add_f32_dpp v32, v32, v32 row_ror:2 row_mask:0xf bank_mask:0xf bound_ctrl:1\n"
        "v_add_f32_dpp v36, v36, v36 row_ror:2 row_mask:0xf bank_mask:0xf bound_ctrl:1\n"
        "v_add_f32_dpp v40, v40, v40 row_ror:2 row_mask:0xf bank_mask:0xf bound_ctrl:1\n"
        "v_add_f32_dpp v32, v32, v32 row_ror:1 row_mask:0xf bank_mask:0xf bound_ctrl:1\n"
        "v_add_f32_dpp v36, v36, v36 row_ror:1 row_mask:0xf bank_mask:0xf bound_ctrl:1\n"
        "v_add_f32_dpp v40, v40, v40 row_ror:1 row_mask:0xf bank_mask:0xf bound_ctrl:1\n"
        "v_mul_f32 v32, 0x3acccccd, v32\n"
        "v_mul_f32 v36, 0x3acccccd, v36\n"
        "v_fma_f32 v36, -v32, v32, v36\n"
        "v_max_f32 v36, 0, v36\n"
        "v_cvt_pk_bf16_f32 v36, 0, v36\n"
        "v_add_f32 v36, 0x3727c5ac, v36\n"
        "v_cvt_pk_bf16_f32 v36, 0, v36\n"
        "v_sqrt_f32 v36, v36\n"
        "v_cvt_pk_bf16_f32 v8, 0, v32\n"
        "v_cvt_pk_bf16_f32 v32, v32, v36\n"
        "v_and_b32 v36, 0xffff0000, v32\n"
        "v_rcp_f32 v36, v36\n"
        "v_fma_f32 v40, -v8, v13, v40\n"
        "v_mul_f32 v40, v40, v36\n"
        "v_cvt_pk_bf16_f32 v40, 0, v40\n"
        "v_ashrrev_i32 v8, 31, v40\n"
        "v_or_b32 v8, 0x80000000, v8\n"
        "v_xor_b32 v40, v40, v8\n"
        "s_mov_b32 exec_lo, 0x00010001\n"
        "s_mov_b32 exec_hi, 0x00010001\n"
        "v_lshrrev_b32 v40, 16, v40\n"
        "v_lshl_or_b32 v40, v40, 11, v7\n"
        "ds_write2_b32 v31, v32, v40 offset1:8\n"
        "s_mov_b64 exec, -1\n"
        "s_waitcnt lgkmcnt(0)\n"
        "s_barrier\n"
        "s_cmp_gt_u32 s28, 1\n"
        "s_cbranch_scc1 .Lpub_xH_h3\n"
        "s_cmp_eq_u32 s28, 1\n"
        "s_cbranch_scc1 .Lpub_sH_h3\n"
        "ds_read2_b32 v[12:13], v49 offset0:8 offset1:24\n"
        "ds_read2_b32 v[14:15], v49 offset0:40 offset1:56\n"
        "v_mov_b32 v11, s31\n"
        "v_cmp_eq_u32 vcc, 0, v51\n"
        "s_waitcnt lgkmcnt(0)\n"
        "v_max3_u32 v10, v12, v13, v14\n"
        "v_med3_u32 v12, v12, v13, v14\n"
        "s_nop 0\n"
        "v_min_u32_dpp v13, v10, v10 row_ror:4 row_mask:0xf bank_mask:0xf bound_ctrl:1\n"
        "v_max_u32_dpp v10, v10, v10 row_ror:4 row_mask:0xf bank_mask:0xf bound_ctrl:1\n"
        "v_max_u32_dpp v12, v12, v12 row_ror:4 row_mask:0xf bank_mask:0xf bound_ctrl:1\n"
        "v_max_u32 v12, v12, v13\n"
        "v_min_u32_dpp v13, v10, v10 row_ror:2 row_mask:0xf bank_mask:0xf bound_ctrl:1\n"
        "v_max_u32_dpp v10, v10, v10 row_ror:2 row_mask:0xf bank_mask:0xf bound_ctrl:1\n"
        "v_max_u32_dpp v12, v12, v12 row_ror:2 row_mask:0xf bank_mask:0xf bound_ctrl:1\n"
        "v_max_u32 v12, v12, v13\n"
        "v_min_u32_dpp v13, v10, v10 row_ror:1 row_mask:0xf bank_mask:0xf bound_ctrl:1\n"
        "v_max_u32_dpp v10, v10, v10 row_ror:1 row_mask:0xf bank_mask:0xf bound_ctrl:1\n"
        "v_max_u32_dpp v12, v12, v12 row_ror:1 row_mask:0xf bank_mask:0xf bound_ctrl:1\n"
        "v_max_u32 v12, v12, v13\n"
        "v_cndmask_b32 v10, v12, v10, vcc\n"
        "s_mov_b64 exec, 0xffff\n"
        "global_atomic_swap_x2 v50, v[10:11], s[14:15] sc1\n"
        "s_mov_b64 exec, -1\n"
        "s_branch .Lpub_xH_h3\n"
        ".Lpub_sH_h3:\n"
        "ds_read_b32 v10, v52\n"
        "v_mov_b32 v11, s31\n"
        "s_mov_b64 exec, 0xffffff\n"
        "s_waitcnt lgkmcnt(0)\n"
        "global_store_dwordx2 v53, v[10:11], s[14:15] sc1\n"
        "s_mov_b64 exec, -1\n"
        ".Lpub_xH_h3:\n"
        "s_barrier\n"
        ".Lh_loop_h3:\n"
        "s_cmp_ge_u32 s48, s49\n"
        "s_cbranch_scc1 .Lh_qx_h3\n"
        "s_sub_u32 s52, s49, 1\n"
        "s_min_u32 s52, s52, s48\n"
        "s_mul_i32 s54, s52, s51\n"
        "s_lshr_b32 s54, s54, 16\n"
        "s_mul_i32 s55, s54, s50\n"
        "s_sub_u32 s52, s52, s55\n"
        "s_lshl_b32 s54, s54, 2\n"
        "s_lshr_b64 s[54:55], s[60:61], s54\n"
        "s_and_b32 s54, s54, 15\n"
        "s_add_u32 s48, s48, s47\n"
        "s_cmp_ge_u32 s54, 10\n"
        "s_cbranch_scc1 .Ljdl_h3\n"
        "s_bfe_u32 s53, s52, 0x20001\n"
        "s_mul_i32 s53, s53, 10\n"
        "s_add_u32 s54, s54, s53\n"
        "s_mul_i32 s54, s54, 55296\n"
        "s_lshr_b32 s53, s52, 3\n"
        "s_add_u32 s53, s53, s89\n"
        "s_lshl_b32 s53, s53, 7\n"
        "s_add_u32 s53, s53, s54\n"
        "s_bitcmp1_b32 s52, 0\n"
        "s_cselect_b32 s56, s22, s20\n"
        "s_cselect_b32 s57, s23, s21\n"
        "v_mov_b32 v47, v44\n"
        "s_branch .Ljxl_h3\n"
        ".Ljdl_h3:\n"
        "s_lshl3_add_u32 s53, s89, s52\n"
        "s_mul_i32 s53, s53, 10\n"
        "s_add_u32 s53, s53, s54\n"
        "s_sub_u32 s53, s53, 10\n"
        "s_lshl_b32 s53, s53, 10\n"
        "s_mov_b64 s[56:57], s[24:25]\n"
        "v_mov_b32 v47, v45\n"
        ".Ljxl_h3:\n"
        "s_add_u32 s56, s56, s53\n"
        "s_addc_u32 s57, s57, 0\n"
        "s_mov_b64 exec, 0xffff\n"
        "global_load_lds_dword v47, s[56:57]\n"
        "s_mov_b64 exec, -1\n"
        "s_branch .Lh_loop_h3\n"
        ".Lh_qx_h3:\n"
        "s_waitcnt vmcnt(0)\n"
        "s_cmp_lg_u32 s2, 255\n"
        "s_cbranch_scc1 .Lh_end_h3\n"
        "v_lshlrev_b32 v4, 6, v0\n"
        "v_add_u32 v4, 4100, v4\n"
        "v_subrev_u32 v5, 256, v0\n"
        "v_min_u32 v5, 159, v5\n"
        "v_lshrrev_b32 v8, 4, v5\n"
        "v_and_b32 v5, 15, v5\n"
        "v_lshlrev_b32 v5, 2, v5\n"
        "v_lshl_add_u32 v5, v8, 11, v5\n"
        "v_add_u32 v5, 1048576, v5\n"
        "v_cmp_gt_u32 vcc, 256, v0\n"
        "s_nop 1\n"
        "v_cndmask_b32 v4, v5, v4, vcc\n"
        "global_load_dword v7, v4, s[14:15] sc1\n"
        "s_waitcnt vmcnt(0)\n"
        "v_cmp_ne_u32 vcc, s31, v7\n"
        "s_nop 0\n"
        "s_cbranch_vccnz .Lep_p_h3\n"
        ".Lep_r_h3:\n"
        "s_barrier\n"
        "s_cmp_lg_u32 s28, 0\n"
        "s_cbranch_scc1 .Lh_end_h3\n"
        "v_mov_b32 v4, 0\n"
        "v_mov_b32 v5, s31\n"
        "s_mov_b64 exec, 1\n"
        "global_store_dword v4, v5, s[14:15] sc1\n"
        "s_mov_b64 exec, -1\n"
        ".Lh_end_h3:\n"
        "s_endpgm\n"
        ".Lrt_p_h3:\n"
        "s_mov_b32 s86, 32768\n"
        ".Lrt_l_h3:\n"
        "s_sleep 2\n"
        "global_load_dwordx4 v[178:181], v228, s[66:67] sc1\n"
        "global_load_dwordx4 v[182:185], v229, s[66:67] sc1\n"
        "global_load_dwordx4 v[186:189], v230, s[66:67] sc1\n"
        "global_load_dwordx4 v[190:193], v231, s[66:67] sc1\n"
        "global_load_dwordx2 v[174:175], v13, s[68:69] sc1\n"
        "s_waitcnt vmcnt(0)\n"
        "v_min3_u32 v176, v175, v179, v181\n"
        "v_min3_u32 v176, v176, v183, v185\n"
        "v_min3_u32 v176, v176, v187, v189\n"
        "v_min3_u32 v176, v176, v191, v193\n"
        "v_max3_u32 v177, v175, v179, v181\n"
        "v_max3_u32 v177, v177, v183, v185\n"
        "v_max3_u32 v177, v177, v187, v189\n"
        "v_max3_u32 v177, v177, v191, v193\n"
        "v_cmp_ne_u32 vcc, s31, v176\n"
        "v_cmp_ne_u32 s[84:85], s31, v177\n"
        "s_nop 0\n"
        "s_or_b64 vcc, vcc, s[84:85]\n"
        "s_cbranch_vccz .Lrt_r_h3\n"
        "s_sub_u32 s86, s86, 1\n"
        "s_cmp_lg_u32 s86, 0\n"
        "s_cbranch_scc1 .Lrt_l_h3\n"
        "s_branch .Lrt_r_h3\n"
        ".LconvEf1_p_h3:\n"
        "s_mov_b32 s86, 32768\n"
        ".LconvEf1_l_h3:\n"
        "s_nop 7\n"
        "s_mov_b64 exec, 0xff\n"
        "global_atomic_or v227, v9, v13, s[68:69] offset:128 sc0 sc1\n"
        "s_mov_b64 exec, -1\n"
        "s_waitcnt vmcnt(0)\n"
        "v_cmp_ne_u32 vcc, s31, v227\n"
        "s_nop 0\n"
        "s_cbranch_vccz .LconvEf1_r_h3\n"
        "s_sub_u32 s86, s86, 1\n"
        "s_cmp_lg_u32 s86, 0\n"
        "s_cbranch_scc1 .LconvEf1_l_h3\n"
        "s_branch .LconvEf1_r_h3\n"
        ".LconvEst_p_h3:\n"
        "s_mov_b32 s86, 32768\n"
        ".LconvEst_l_h3:\n"
        "s_nop 7\n"
        "s_sleep 2\n"
        "global_load_dwordx4 v[244:247], v8, s[48:49] sc1\n"
        "s_waitcnt vmcnt(0)\n"
        "v_cmp_ne_u32 vcc, s31, v245\n"
        "v_cmp_ne_u32 s[84:85], s31, v247\n"
        "s_nop 0\n"
        "s_or_b64 vcc, vcc, s[84:85]\n"
        "s_cbranch_vccz .LconvEst_r_h3\n"
        "s_sub_u32 s86, s86, 1\n"
        "s_cmp_lg_u32 s86, 0\n"
        "s_cbranch_scc1 .LconvEst_l_h3\n"
        "s_branch .LconvEst_r_h3\n"
        ".LconvLf1_p_h3:\n"
        "s_mov_b32 s86, 32768\n"
        ".LconvLf1_l_h3:\n"
        "s_nop 7\n"
        "s_mov_b64 exec, 0xffff\n"
        "global_atomic_or v227, v9, v13, s[68:69] offset:128 sc0 sc1\n"
        "s_mov_b64 exec, -1\n"
        "s_waitcnt vmcnt(0)\n"
        "v_cmp_ne_u32 vcc, s31, v227\n"
        "s_nop 0\n"
        "s_cbranch_vccz .LconvLf1_r_h3\n"
        "s_sub_u32 s86, s86, 1\n"
        "s_cmp_lg_u32 s86, 0\n"
        "s_cbranch_scc1 .LconvLf1_l_h3\n"
        "s_branch .LconvLf1_r_h3\n"
        ".LconvLst_p_h3:\n"
        "s_mov_b32 s86, 32768\n"
        ".LconvLst_l_h3:\n"
        "s_nop 7\n"
        "s_sleep 2\n"
        "global_load_dwordx4 v[244:247], v8, s[48:49] sc1\n"
        "s_waitcnt vmcnt(0)\n"
        "v_cmp_ne_u32 vcc, s31, v245\n"
        "v_cmp_ne_u32 s[84:85], s31, v247\n"
        "s_nop 0\n"
        "s_or_b64 vcc, vcc, s[84:85]\n"
        "s_cbranch_vccz .LconvLst_r_h3\n"
        "s_sub_u32 s86, s86, 1\n"
        "s_cmp_lg_u32 s86, 0\n"
        "s_cbranch_scc1 .LconvLst_l_h3\n"
        "s_branch .LconvLst_r_h3\n"
        ".Lrsel_p_h3:\n"
        "s_mov_b32 s86, 32768\n"
        ".Lrsel_l_h3:\n"
        "s_nop 7\n"
        "s_sleep 4\n"
        "global_load_dwordx2 v[226:227], v13, s[78:79] sc1\n"
        "s_waitcnt vmcnt(0)\n"
        "v_cmp_ne_u32 vcc, s31, v227\n"
        "s_nop 0\n"
        "s_cbranch_vccz .Lrsel_r_h3\n"
        "s_sub_u32 s86, s86, 1\n"
        "s_cmp_lg_u32 s86, 0\n"
        "s_cbranch_scc1 .Lrsel_l_h3\n"
        "s_branch .Lrsel_r_h3\n"
        ".Lkeys_p_h3:\n"
        "s_mov_b32 s86, 32768\n"
        ".Lkeys_l_h3:\n"
        "s_nop 7\n"
        "global_atomic_or_x2 v[8:9], v131, v[132:133], s[14:15] offset:0 sc0 sc1\n"
        "global_atomic_or_x2 v[10:11], v131, v[132:133], s[14:15] offset:512 sc0 sc1\n"
        "global_atomic_or_x2 v[12:13], v131, v[132:133], s[14:15] offset:1024 sc0 sc1\n"
        "s_waitcnt vmcnt(0)\n"
        "v_cmp_ne_u32 vcc, s31, v9\n"
        "v_cmp_ne_u32 s[84:85], s31, v11\n"
        "s_nop 0\n"
        "s_or_b64 vcc, vcc, s[84:85]\n"
        "v_cmp_ne_u32 s[84:85], s31, v13\n"
        "s_nop 0\n"
        "s_or_b64 vcc, vcc, s[84:85]\n"
        "s_cbranch_vccz .Lkeys_r_h3\n"
        "s_sub_u32 s86, s86, 1\n"
        "s_cmp_lg_u32 s86, 0\n"
        "s_cbranch_scc1 .Lkeys_l_h3\n"
        "s_branch .Lkeys_r_h3\n"
        ".Lfst_p_h3:\n"
        "s_mov_b32 s86, 32768\n"
        ".Lfst_l_h3:\n"
        "s_nop 7\n"
        "global_load_dwordx2 v[16:17], v18, s[66:67] sc1\n"
        "s_waitcnt vmcnt(0)\n"
        "v_cmp_ne_u32 vcc, s31, v17\n"
        "s_nop 0\n"
        "s_cbranch_vccz .Lfst_r_h3\n"
        "s_sub_u32 s86, s86, 1\n"
        "s_cmp_lg_u32 s86, 0\n"
        "s_cbranch_scc1 .Lfst_l_h3\n"
        "s_branch .Lfst_r_h3\n"
        ".Lep_p_h3:\n"
        "s_mov_b32 s86, 32768\n"
        ".Lep_l_h3:\n"
        "s_nop 7\n"
        "s_sleep 8\n"
        "global_load_dword v7, v4, s[14:15] sc1\n"
        "s_waitcnt vmcnt(0)\n"
        "v_cmp_ne_u32 vcc, s31, v7\n"
        "s_nop 0\n"
        "s_cbranch_vccz .Lep_r_h3\n"
        "s_sub_u32 s86, s86, 1\n"
        "s_cmp_lg_u32 s86, 0\n"
        "s_cbranch_scc1 .Lep_l_h3\n"
        "s_branch .Lep_r_h3\n"
        ".Lend_h3:\n"
        :
        : "{s[0:1]}"(__builtin_amdgcn_kernarg_segment_ptr()), "{s2}"(blockIdx.x), "{v0}"(threadIdx.x), "s"((__attribute__((address_space(3))) char*)smem)
        : "v1", "v2", "v3", "v4", "v5", "v6", "v7", "v8", "v9", "v10", "v11", "v12", "v13", "v14", "v15", "v16", "v17", "v18", "v19", "v20", "v21", "v22", "v23", "v24", "v25", "v26", "v27", "v28", "v29", "v30", "v31", "v32", "v33", "v34", "v35", "v36", "v37", "v38", "v39", "v40", "v41", "v42", "v43", "v44", "v45", "v46", "v47", "v48", "v49", "v50", "v51", "v52", "v53", "v54", "v55", "v56", "v57", "v58", "v59", "v60", "v61", "v62", "v63", "v64", "v65", "v66", "v67", "v68", "v69", "v70", "v71", "v72", "v73", "v74", "v75", "v76", "v77", "v78", "v79", "v80", "v81", "v82", "v83", "v84", "v85", "v86", "v87", "v88", "v89", "v90", "v91", "v92", "v93", "v94", "v95", "v96", "v97", "v98", "v99", "v100", "v101", "v102", "v103", "v104", "v105", "v106", "v107", "v108", "v109", "v110", "v111", "v112", "v113", "v114", "v115", "v116", "v117", "v118", "v119", "v120", "v121", "v122", "v123", "v124", "v125", "v126", "v127", "v128", "v129", "v130", "v131", "v132", "v133", "v134", "v135", "v136", "v137", "v138", "v139", "v140", "v141", "v142", "v143", "v144", "v145", "v146", "v147", "v148", "v149", "v150", "v151", "v152", "v153", "v154", "v155", "v156", "v157", "v158", "v159", "v160", "v161", "v162", "v163", "v164", "v165", "v166", "v167", "v168", "v169", "v170", "v171", "v172", "v173", "v174", "v175", "v176", "v177", "v178", "v179", "v180", "v181", "v182", "v183", "v184", "v185", "v186", "v187", "v188", "v189", "v190", "v191", "v192", "v193", "v194", "v195", "v196", "v197", "v198", "v199", "v200", "v201", "v202", "v203", "v204", "v205", "v206", "v207", "v208", "v209", "v210", "v211", "v212", "v213", "v214", "v215", "v216", "v217", "v218", "v219", "v220", "v221", "v222", "v223", "v224", "v225", "v226", "v227", "v228", "v229", "v230", "v231", "v232", "v233", "v234", "v235", "v236", "v237", "v238", "v239", "v240", "v241", "v242", "v243", "v244", "v245", "v246", "v247", "v248", "v249", "v250", "v251", "s3", "s4", "s5", "s6", "s7", "s8", "s9", "s10", "s11", "s12", "s13", "s14", "s15", "s16", "s17", "s18", "s19", "s20", "s21", "s22", "s23", "s24", "s25", "s26", "s27", "s28", "s29", "s30", "s31", "s33", "s34", "s35", "s36", "s37", "s38", "s39", "s40", "s41", "s42", "s43", "s44", "s45", "s46", "s47", "s48", "s49", "s50", "s51", "s52", "s53", "s54", "s55", "s56", "s57", "s58", "s59", "s60", "s61", "s62", "s63", "s64", "s65", "s66", "s67", "s68", "s69", "s70", "s71", "s72", "s73", "s74", "s75", "s76", "s77", "s78", "s79", "s80", "s81", "s82", "s83", "s84", "s85", "s86", "s87", "s88", "s89", "s90", "s91", "s92", "s93", "s94", "s95", "s96", "s97", "vcc", "scc", "memory");
}

extern "C" void kernel_launch(void* const* d_in, const int* in_sizes, int n_in, void* d_out, int out_size, void* d_ws, size_t ws_size, hipStream_t stream) {
    Args a;
    a.q = d_in[0]; a.k = d_in[1]; a.v = d_in[2]; a.sk = d_in[3]; a.h = d_in[4]; a.dsk = d_in[5];
    a.wr = d_in[7]; a.wg = d_in[8]; a.wu = d_in[9]; a.wd = d_in[10]; a.out = d_out; a.ws = d_ws;
    h3_mod_kernel<<<256, 512, 0, stream>>>(a);
}
```
